```python
import torch

def _softmax(x, axis=-1):
    return torch.softmax(x, dim=axis)
def _gelu(x):
    return torch.nn.functional.gelu(x, approximate="tanh")
def _silu(x):
    return torch.nn.functional.silu(x)
def _sigmoid(x):
    return torch.sigmoid(x)
def _layernorm(x, axis=-1, eps=1e-5):
    m = x.mean(dim=axis, keepdim=True)
    v = x.var(dim=axis, keepdim=True, unbiased=False)
    return (x - m) / torch.sqrt(v + eps)
def _scatter_add(tgt, idx, src, axis=0):
    return tgt.clone().index_add_(axis, idx, src)
def _segment_sum(src, seg, n):
    out = torch.zeros((n,) + src.shape[1:], dtype=src.dtype)
    return out.index_add_(0, seg, src)
def _pad(x, pw):
    flat = tuple(v for pair in reversed(pw) for v in pair)
    return torch.nn.functional.pad(x, flat)

kernel_name = 'concat_heads__gqa_block_cached__gqa_block_cached__near_ridge'

def setup_inputs():

    _w = (lambda a: (lambda t: t.to(torch.bfloat16) if t.is_floating_point() else t)(torch.as_tensor(a)))
    def _A_setup():
        B, Sq, D = 8, 128, 640
        Hq, Hkv, Sk, Dh = 32, 32, 512, 192
        assert Hq % Hkv == 0, (Hq, Hkv)
        return {
            "x": _w((lambda *s: torch.randn(*s, dtype=torch.bfloat16) * 0.8)(B, Sq, D)),
            "k_cache": _w((lambda *s: torch.randn(*s, dtype=torch.bfloat16) * 0.8)(B, Hkv, Sk, Dh)),
            "v_cache": _w((lambda *s: torch.randn(*s, dtype=torch.bfloat16) * 0.05)(B, Hkv, Sk, Dh)),
            "wq": _w((lambda *s: torch.randn(*s, dtype=torch.bfloat16) * 0.8)(D, Hq * Dh) * (D ** -0.5)),
            "wo": _w((lambda *s: torch.randn(*s, dtype=torch.bfloat16) * 0.05)(Hq * Dh, D) * ((Hq * Dh) ** -0.5)),
        }
    _A_in = _A_setup()

    def _B_setup():
        B, Sq, D = 8, 128, 640
        Hq, Hkv, Sk, Dh = 32, 32, 512, 192
        assert Hq % Hkv == 0, (Hq, Hkv)
        return {
            "x": _w((lambda *s: torch.randn(*s, dtype=torch.bfloat16) * 0.8)(B, Sq, D)),
            "k_cache": _w((lambda *s: torch.randn(*s, dtype=torch.bfloat16) * 0.8)(B, Hkv, Sk, Dh)),
            "v_cache": _w((lambda *s: torch.randn(*s, dtype=torch.bfloat16) * 0.05)(B, Hkv, Sk, Dh)),
            "wq": _w((lambda *s: torch.randn(*s, dtype=torch.bfloat16) * 0.8)(D, Hq * Dh) * (D ** -0.5)),
            "wo": _w((lambda *s: torch.randn(*s, dtype=torch.bfloat16) * 0.05)(Hq * Dh, D) * ((Hq * Dh) ** -0.5)),
        }
    _B_in = _B_setup()

    return {
        "A_x": _A_in["x"],
        "A_k_cache": _A_in["k_cache"],
        "A_v_cache": _A_in["v_cache"],
        "A_wq": _A_in["wq"],
        "A_wo": _A_in["wo"],
        "B_x": _B_in["x"],
        "B_k_cache": _B_in["k_cache"],
        "B_v_cache": _B_in["v_cache"],
        "B_wq": _B_in["wq"],
        "B_wo": _B_in["wo"],
    }

def reference(A_x, A_k_cache, A_v_cache, A_wq, A_wo, B_x, B_k_cache, B_v_cache, B_wq, B_wo):
    def _A_ref(x, k_cache, v_cache, wq, wo):
        B, Sq, D = x.shape
        Hq, Dh = 32, 192
        Hkv = k_cache.shape[1]
        rep = Hq // Hkv
        q = (lambda x, *d: x.permute(*d))((x @ wq).reshape(B, Sq, Hq, Dh), 0, 2, 1, 3)
        k = (lambda x, n, axis: torch.repeat_interleave(x, n, dim=axis))(k_cache, rep, axis=1)
        v = (lambda x, n, axis: torch.repeat_interleave(x, n, dim=axis))(v_cache, rep, axis=1)
        scores = torch.einsum("bhqd,bhkd->bhqk", q, k) * (Dh ** -0.5)
        p = _softmax(scores, axis=-1)
        o = torch.einsum("bhqk,bhkd->bhqd", p, v)
        return (lambda x, *d: x.permute(*d))(o, 0, 2, 1, 3).reshape(B, Sq, Hq * Dh) @ wo

    def _B_ref(x, k_cache, v_cache, wq, wo):
        B, Sq, D = x.shape
        Hq, Dh = 32, 192
        Hkv = k_cache.shape[1]
        rep = Hq // Hkv
        q = (lambda x, *d: x.permute(*d))((x @ wq).reshape(B, Sq, Hq, Dh), 0, 2, 1, 3)
        k = (lambda x, n, axis: torch.repeat_interleave(x, n, dim=axis))(k_cache, rep, axis=1)
        v = (lambda x, n, axis: torch.repeat_interleave(x, n, dim=axis))(v_cache, rep, axis=1)
        scores = torch.einsum("bhqd,bhkd->bhqk", q, k) * (Dh ** -0.5)
        p = _softmax(scores, axis=-1)
        o = torch.einsum("bhqk,bhkd->bhqd", p, v)
        return (lambda x, *d: x.permute(*d))(o, 0, 2, 1, 3).reshape(B, Sq, Hq * Dh) @ wo

    a = _A_ref(A_x, A_k_cache, A_v_cache, A_wq, A_wo)
    b = _B_ref(B_x, B_k_cache, B_v_cache, B_wq, B_wo)
    return torch.concatenate([a, b], axis=-1)
```

```cpp
#include <hip/hip_runtime.h>
#include <hip/hip_bf16.h>
#include <stdint.h>

namespace naive {
constexpr int NB = 8, SQ = 128, DM = 640, NH = 32, SK = 512, DH = 192, HD = NH * DH;
typedef unsigned short u16;
__device__ __forceinline__ float bf2f(u16 v) { return __uint_as_float(((unsigned)v) << 16); }
__device__ __forceinline__ u16 f2bf(float f) { unsigned u = __float_as_uint(f); u += 0x7fffu + ((u >> 16) & 1u); return (u16)(u >> 16); }

__global__ void k_q(const u16* x, const u16* wq, u16* q) {
  int col = blockIdx.x * 256 + threadIdx.x; int row = blockIdx.y;
  float acc = 0.f;
  for (int k = 0; k < DM; ++k) acc += bf2f(x[row * DM + k]) * bf2f(wq[(size_t)k * HD + col]);
  q[(size_t)row * HD + col] = f2bf(acc);
}
__global__ void k_attn(const u16* q, const u16* kc, const u16* vc, u16* o) {
  int qi = blockIdx.x, h = blockIdx.y, b = blockIdx.z, t = threadIdx.x;
  __shared__ float s[SK]; __shared__ float red[256]; __shared__ float qs[DH];
  const u16* qr = q + (size_t)(b * SQ + qi) * HD + h * DH;
  if (t < DH) qs[t] = bf2f(qr[t]);
  __syncthreads();
  const u16* K = kc + (size_t)(b * NH + h) * SK * DH; const u16* V = vc + (size_t)(b * NH + h) * SK * DH;
  float scale = 0.07216878364870322f;
  float mx = -INFINITY;
  for (int j = t; j < SK; j += 256) { float a = 0.f; for (int d = 0; d < DH; ++d) a += qs[d] * bf2f(K[j * DH + d]);
    a = bf2f(f2bf(a)); a = bf2f(f2bf(a * scale)); s[j] = a; mx = fmaxf(mx, a); }
  red[t] = mx; __syncthreads();
  for (int st = 128; st > 0; st >>= 1) { if (t < st) red[t] = fmaxf(red[t], red[t + st]); __syncthreads(); }
  mx = red[0]; __syncthreads();
  float sum = 0.f;
  for (int j = t; j < SK; j += 256) { float e = expf(s[j] - mx); s[j] = e; sum += e; }
  red[t] = sum; __syncthreads();
  for (int st = 128; st > 0; st >>= 1) { if (t < st) red[t] += red[t + st]; __syncthreads(); }
  sum = red[0]; __syncthreads();
  for (int j = t; j < SK; j += 256) s[j] = bf2f(f2bf(s[j] / sum));
  __syncthreads();
  if (t < DH) { float a = 0.f; for (int j = 0; j < SK; ++j) a += s[j] * bf2f(V[j * DH + t]);
    o[(size_t)(b * SQ + qi) * HD + h * DH + t] = f2bf(a); }
}
__global__ void k_o(const u16* o, const u16* wo, u16* out, int coff) {
  int col = blockIdx.x * 128 + threadIdx.x; int row = blockIdx.y;
  float acc = 0.f;
  for (int k = 0; k < HD; ++k) acc += bf2f(o[(size_t)row * HD + k]) * bf2f(wo[(size_t)k * DM + col]);
  out[(size_t)row * (2 * DM) + coff + col] = f2bf(acc);
}
static void run(void* const* d_in, void* d_out, void* d_ws, hipStream_t stream) {
  u16* q = (u16*)d_ws; u16* o = q + (size_t)NB * SQ * HD;
  for (int br = 0; br < 2; ++br) {
    const u16* x = (const u16*)d_in[br * 5 + 0]; const u16* kc = (const u16*)d_in[br * 5 + 1]; const u16* vc = (const u16*)d_in[br * 5 + 2];
    const u16* wq = (const u16*)d_in[br * 5 + 3]; const u16* wo = (const u16*)d_in[br * 5 + 4];
    k_q<<<dim3(HD / 256, NB * SQ), 256, 0, stream>>>(x, wq, q);
    k_attn<<<dim3(SQ, NH, NB), 256, 0, stream>>>(q, kc, vc, o);
    k_o<<<dim3(DM / 128, NB * SQ), 128, 0, stream>>>(o, wo, (u16*)d_out, br * DM);
  }
}
}
extern "C" void kernel_launch(void* const* d_in, const int* in_sizes, int n_in, void* d_out, int out_size, void* d_ws, size_t ws_size, hipStream_t stream) {
  naive::run(d_in, d_out, d_ws, stream);
}
```

```cpp
#include <hip/hip_runtime.h>
#include <stdint.h>

typedef unsigned u32;
typedef unsigned long long u64;
typedef __bf16 bf16x8 __attribute__((ext_vector_type(8)));
typedef __bf16 bf16x2 __attribute__((ext_vector_type(2)));
typedef short s16x4 __attribute__((ext_vector_type(4)));
typedef short s16x8 __attribute__((ext_vector_type(8)));
typedef float f32x2 __attribute__((ext_vector_type(2)));
typedef float f32x16 __attribute__((ext_vector_type(16)));
typedef u32 u32x4 __attribute__((ext_vector_type(4)));
#define LAS __attribute__((address_space(3)))
#define GAS __attribute__((address_space(1)))
#define DI __device__ __forceinline__
typedef __amdgpu_buffer_rsrc_t rsrc_t;
typedef GAS u64 gu64;

constexpr int NB = 8, SQ = 128, DM = 640, NH = 32, SK = 512, DH = 192, HD = NH * DH;
constexpr u32 SLOT = 24576;
constexpr u32 WQS = 12288;
constexpr u32 XOFF = 49152;
constexpr u32 WOS = 20480;
constexpr u32 LDS_BYTES = 81920;
constexpr size_t WS_E = 0;
constexpr size_t WS_ENTER = 4096;
constexpr size_t WS_FA = 8192;
constexpr size_t WS_FO = 32768;
constexpr size_t WS_FP = 65536;
constexpr size_t WS_O = 131072;
constexpr size_t WS_O_BYTES = (size_t)2 * NB * NH * 4 * 12288;
constexpr size_t WS_P = WS_O + WS_O_BYTES;
#define P_F32 0
constexpr u32 PB = P_F32 ? 2 : 1;
constexpr size_t WS_P_BYTES = (size_t)2 * NB * 4 * 4 * 10 * 8 * 1024 * PB;

struct Params {
  const void* x[2]; const void* kc[2]; const void* vc[2]; const void* wq[2]; const void* wo[2];
  void* out; void* ws;
};

DI u32 rfl(u32 v) { return __builtin_amdgcn_readfirstlane(v); }
DI rsrc_t mk_rsrc(const void* p, u32 bytes) { return __builtin_amdgcn_make_buffer_rsrc((void*)p, (short)0, (int)bytes, 0x00020000); }
DI u32 cvtpk(float lo, float hi) { f32x2 v = {lo, hi}; bf16x2 b = __builtin_convertvector(v, bf16x2); return __builtin_bit_cast(u32, b); }
DI bf16x8 pack8(const f32x16& a, int base, float sc) {
  u32x4 w = {cvtpk(a[base + 0] * sc, a[base + 1] * sc), cvtpk(a[base + 2] * sc, a[base + 3] * sc), cvtpk(a[base + 4] * sc, a[base + 5] * sc), cvtpk(a[base + 6] * sc, a[base + 7] * sc)};
  return __builtin_bit_cast(bf16x8, w);
}
DI bf16x8 pack8n(const f32x16& a, int base) {
  u32x4 w = {cvtpk(a[base + 0], a[base + 1]), cvtpk(a[base + 2], a[base + 3]), cvtpk(a[base + 4], a[base + 5]), cvtpk(a[base + 6], a[base + 7])};
  return __builtin_bit_cast(bf16x8, w);
}
#define MFMA(a, b, c) __builtin_amdgcn_mfma_f32_32x32x16_bf16((a), (b), (c), 0, 0, 0)
#define PIN() __builtin_amdgcn_sched_barrier(0)
#define MF(acc, a, b) do { acc = MFMA((a), (b), acc); asm volatile("" : "+v"(acc)); } while (0)

template <bool NT> DI void dma16(u32 lds, u32 voff, rsrc_t srd, u32 soff) {
  if constexpr (NT) asm volatile("s_mov_b32 m0, %0\n\ts_nop 0\n\tbuffer_load_dwordx4 %1, %2, %3 offen nt lds" :: "s"(lds), "v"(voff), "s"(srd), "s"(soff));
  else              asm volatile("s_mov_b32 m0, %0\n\ts_nop 0\n\tbuffer_load_dwordx4 %1, %2, %3 offen lds" :: "s"(lds), "v"(voff), "s"(srd), "s"(soff));
}
template <int IMM> DI void ld16_sc1(u32x4& dst, u32 voff, rsrc_t srd, u32 soff) {
  asm volatile("buffer_load_dwordx4 %0, %1, %2, %3 offen offset:%4 sc1" : "=v"(dst) : "v"(voff), "s"(srd), "s"(soff), "i"(IMM));
}
template <int IMM> DI void st16_sc1(const u32x4& src, u32 voff, rsrc_t srd, u32 soff) {
  asm volatile("buffer_store_dwordx4 %0, %1, %2, %3 offen offset:%4 sc1\n\ts_nop 1" :: "v"(src), "v"(voff), "s"(srd), "s"(soff), "i"(IMM) : "memory");
}
template <int N> DI void wait_bar() { asm volatile("s_waitcnt vmcnt(%0)\n\ts_barrier" :: "i"(N) : "memory"); }
template <int N> DI void wait_bar_lgkm() { asm volatile("s_waitcnt vmcnt(%0) lgkmcnt(0)\n\ts_barrier" :: "i"(N) : "memory"); }
DI bf16x8 lds_b128(u32 addr) { return *(const LAS bf16x8*)(size_t)addr; }
DI s16x4 lds_tr(u32 a) { return __builtin_amdgcn_ds_read_tr16_b64_v4i16((LAS s16x4*)(size_t)a); }
DI bf16x8 comb(s16x4 lo, s16x4 hi) { s16x8 v = __builtin_shufflevector(lo, hi, 0, 1, 2, 3, 4, 5, 6, 7); return __builtin_bit_cast(bf16x8, v); }
DI void put_tag(gu64* word, u64 tag) { __hip_atomic_store(word, tag, __ATOMIC_RELAXED, __HIP_MEMORY_SCOPE_AGENT); }
DI void poll_tags(gu64* words, u32 nmask, u32 lane, u64 tag) {
  u32 spins = 0;
  gu64* wp = words + (lane & nmask);
  while (true) {
    const u64 v = __hip_atomic_load(wp, __ATOMIC_RELAXED, __HIP_MEMORY_SCOPE_AGENT);
    if (__builtin_amdgcn_ballot_w64(v != tag) == 0ull) break;
    __builtin_amdgcn_s_sleep(2);
    if (++spins > (1u << 18)) break;
  }
}

#define KV_NT 1
#define W_NT 0
#define X_NT 0
#define THROTTLE_IT 12
#define USE_PRIO 1

__global__ __launch_bounds__(256, 2) void fused_kernel(Params p) {
  __shared__ __attribute__((aligned(1024))) char smem[LDS_BYTES];
  const u32 ldsb = (u32)(size_t)(LAS char*)smem;
  const u32 tid = threadIdx.x, lane = tid & 63u, w = rfl(tid >> 6);
  const u32 r = lane & 31u, hh = lane >> 5;
  const u32 blk = blockIdx.x, br = blk >> 8, idx = blk & 255u, j = idx & 7u, sl = idx >> 3, b = sl >> 2, i = sl & 3u, h = 4u * j + i;
  char* const wsb = (char*)p.ws;
  if (USE_PRIO && br == 0) __builtin_amdgcn_s_setprio(2);
  u64 ev;
  { const u32 vz = 0; asm volatile("global_load_dwordx2 %0, %1, %2 sc1" : "=v"(ev) : "v"(vz), "s"(wsb)); }

  const rsrc_t srdW = mk_rsrc((const char*)p.wq[br] + h * (DH * 2), (u32)(DM * HD * 2) - h * (DH * 2));
  const rsrc_t srdX = mk_rsrc((const char*)p.x[br] + (size_t)b * SQ * DM * 2, SQ * DM * 2);
  const rsrc_t srdK = mk_rsrc((const char*)p.kc[br] + (size_t)(b * NH + h) * SK * DH * 2, SK * DH * 2);
  const rsrc_t srdV = mk_rsrc((const char*)p.vc[br] + (size_t)(b * NH + h) * SK * DH * 2, SK * DH * 2);

  u32 voffW[3], voffK[3], voffV[3], voffX[2];
#pragma unroll
  for (int v = 0; v < 3; ++v) {
    const u32 z = 64u * v + lane, rr = z / 24u, ph = z % 24u;
    const u32 cV = ph ^ (((rr >> 1) & 1u) << 2);
    const u32 cK = (ph & ~7u) | ((ph & 7u) ^ (((w & 1u) << 2) | (rr >> 1)));
    voffW[v] = rr * (HD * 2) + cV * 16u;
    voffV[v] = rr * 384u + cV * 16u;
    voffK[v] = rr * 384u + cK * 16u;
  }
#pragma unroll
  for (int v = 0; v < 2; ++v) voffX[v] = (lane >> 3) * (DM * 2) + ((lane & 7u) ^ ((u32)(v << 2) | (lane >> 4))) * 16u;
  const u32 blkb = (lane >> 4) & 1u, q4 = (lane & 15u) >> 2, pp = lane & 3u, e = (q4 >> 1) & 1u;
  const u32 bW0 = ldsb + (8u * hh + q4) * 384u + (4u * e + 2u * blkb + (pp & 1u)) * 16u + 8u * (pp >> 1), bW1 = bW0 ^ 64u;
  const u32 baseV0 = ldsb + 12288u + (4u * hh + q4) * 384u + (4u * e + 2u * blkb + (pp >> 1)) * 16u + 8u * (pp & 1u);
  const u32 baseK0 = ldsb + r * 384u + ((hh ^ ((r >> 1) & 7u)) << 4);
  const u32 baseX0 = ldsb + XOFF + w * 4096u + r * 128u + ((hh ^ ((r >> 1) & 7u)) << 4);

  const u32 c0 = (b + (b >> 2) + 2u * i + (i >> 1)) % 10u;
  auto chunk_of = [&](u32 c) { u32 t = c + c0; return t >= 10u ? t - 10u : t; };
  auto wq_piece = [&](u32 slot, u32 so, int v) {
    dma16<W_NT>(ldsb + slot * WQS + w * 3072u + v * 1024u, voffW[v], srdW, so); };
  auto wq_so = [&](u32 t) { return (chunk_of(t >> 1) * 64u + (t & 1u) * 32u + 8u * w) * (HD * 2); };
  auto x_piece = [&](u32 par, u32 so, int v) {
    dma16<X_NT>(ldsb + XOFF + par * 16384u + w * 4096u + v * 1024u, voffX[v & 1], srdX, so + v * 8u * (DM * 2)); };
  auto x_so = [&](u32 c) { return 32u * w * (DM * 2) + chunk_of(c) * 128u; };
  auto k_piece = [&](u32 t, u32 slot_off, int v) { dma16<KV_NT>(ldsb + slot_off + w * 3072u + v * 1024u, voffK[v], srdK, t * 12288u + w * 3072u); };
  auto v_piece = [&](u32 t, u32 slot_off, int v) { dma16<KV_NT>(ldsb + slot_off + 12288u + w * 3072u + v * 1024u, voffV[v], srdV, t * 12288u + w * 3072u); };

  f32x16 acc[6];
#pragma unroll
  for (int t = 0; t < 6; ++t) acc[t] = f32x16{};
  {
    const u32 s0 = wq_so(0), s1 = wq_so(1), s2 = wq_so(2), s3 = wq_so(3), x0 = x_so(0), x1 = x_so(1);
#pragma unroll
    for (int v = 0; v < 3; ++v) wq_piece(0, s0, v);
#pragma unroll
    for (int v = 0; v < 4; ++v) x_piece(0, x0, v);
#pragma unroll
    for (int v = 0; v < 3; ++v) wq_piece(1, s1, v);
#pragma unroll
    for (int v = 0; v < 3; ++v) wq_piece(2, s2, v);
#pragma unroll
    for (int v = 0; v < 4; ++v) x_piece(1, x1, v);
#pragma unroll
    for (int v = 0; v < 3; ++v) wq_piece(3, s3, v);
  }
  asm volatile("s_waitcnt vmcnt(13)\n\ts_barrier" : "+v"(ev) :: "memory");
  const u64 tag = (((u64)rfl((u32)(ev >> 32)) << 32) | rfl((u32)ev)) + 1ull;
  if (tid == 0) put_tag((gu64*)(wsb + WS_ENTER) + blk, tag);
  auto rdA = [&](int slot, int ks, int m, int hi) { return lds_tr(((m & 1) ? bW1 : bW0) + slot * WQS + ks * (16 * 384) + (m >> 1) * 128 + hi * (4 * 384)); };
  auto rdX = [&](int par, int xks) { return lds_b128((baseX0 ^ (u32)(xks << 5)) + par * 16384); };
  s16x4 nlo[6], nhi[6];
  bf16x8 Xn;
  auto qp_ks0 = [&](auto Sc, const s16x4 (&lo)[6], const s16x4 (&hi)[6], const bf16x8& X) {
    constexpr int S = decltype(Sc)::value, PAR = S >> 1, XK = (S & 1) * 2;
    MF(acc[0], comb(lo[0], hi[0]), X); nlo[0] = rdA(S, 1, 0, 0); nhi[0] = rdA(S, 1, 0, 1); nlo[1] = rdA(S, 1, 1, 0); PIN();
    MF(acc[1], comb(lo[1], hi[1]), X); nhi[1] = rdA(S, 1, 1, 1); nlo[2] = rdA(S, 1, 2, 0); nhi[2] = rdA(S, 1, 2, 1); PIN();
    MF(acc[2], comb(lo[2], hi[2]), X); nlo[3] = rdA(S, 1, 3, 0); nhi[3] = rdA(S, 1, 3, 1); nlo[4] = rdA(S, 1, 4, 0); PIN();
    MF(acc[3], comb(lo[3], hi[3]), X); nhi[4] = rdA(S, 1, 4, 1); nlo[5] = rdA(S, 1, 5, 0); nhi[5] = rdA(S, 1, 5, 1); PIN();
    MF(acc[4], comb(lo[4], hi[4]), X); Xn = rdX(PAR, XK + 1); PIN();
    MF(acc[5], comb(lo[5], hi[5]), X); PIN();
  };
  {
    s16x4 Alo[6], Ahi[6];
#pragma unroll
    for (int m = 0; m < 6; ++m) { Alo[m] = rdA(0, 0, m, 0); Ahi[m] = rdA(0, 0, m, 1); }
    const bf16x8 X = rdX(0, 0);
    PIN();
    qp_ks0(std::integral_constant<int, 0>{}, Alo, Ahi, X);
  }
  auto qp_tile = [&](auto TTc, auto LASTc, u32 per) {
    constexpr int TT = decltype(TTc)::value; constexpr bool LAST = decltype(LASTc)::value;
    constexpr int PAR = TT >> 1;
    if constexpr (!LAST) { if constexpr (TT & 1) wait_bar_lgkm<6>(); else wait_bar_lgkm<10>(); }
    else { if constexpr (TT == 0) wait_bar_lgkm<10>(); else if constexpr (TT < 3) wait_bar_lgkm<6>(); else wait_bar_lgkm<63>(); }
    PIN();
    const u32 tn = 4u * per + TT + 4u;
    u32 sw = 0, sx = 0;
    if constexpr (!LAST) { sw = wq_so(tn); if constexpr (TT & 1) sx = x_so(tn >> 1); }
    constexpr int NS = (TT + 1) & 3, NPAR = NS >> 1, NXK = (NS & 1) * 2;
    constexpr bool MORE = !(LAST && TT == 3);
    s16x4 Alo[6], Ahi[6]; bf16x8 X;
#pragma unroll
    for (int m = 0; m < 6; ++m) {
      MF(acc[m], comb(nlo[m], nhi[m]), Xn);
      if constexpr (MORE) { Alo[m] = rdA(NS, 0, m, 0); Ahi[m] = rdA(NS, 0, m, 1); if (m == 0) X = rdX(NPAR, NXK); }
      if constexpr (!LAST) {
        if constexpr (TT & 1) {
          if (m == 0) { x_piece(PAR, sx, 0); } if (m == 1) { x_piece(PAR, sx, 1); x_piece(PAR, sx, 2); } if (m == 2) x_piece(PAR, sx, 3);
          if (m >= 3) wq_piece(TT, sw, m - 3);
        } else { if (m & 1) wq_piece(TT, sw, m >> 1); }
      } else {
        if constexpr (TT == 0) { if (m & 1) k_piece(0, 0, m >> 1); }
        if constexpr (TT == 1) { if (m & 1) v_piece(0, 0, m >> 1); }
        if constexpr (TT == 2) { if (m & 1) k_piece(1, SLOT, m >> 1); }
        if constexpr (TT == 3) { if (m < 3) v_piece(1, SLOT, m); else k_piece(2, 2 * SLOT, m - 3); }
      }
      PIN();
    }
    if constexpr (MORE) qp_ks0(std::integral_constant<int, NS>{}, Alo, Ahi, X);
  };
  using std::integral_constant;
#define IC(n) integral_constant<int, n>{}
#define BC(n) integral_constant<bool, n>{}
  for (u32 per = 0; per < 4; ++per) { qp_tile(IC(0), BC(false), per); qp_tile(IC(1), BC(false), per); qp_tile(IC(2), BC(false), per); qp_tile(IC(3), BC(false), per); }
  qp_tile(IC(0), BC(true), 4); qp_tile(IC(1), BC(true), 4); qp_tile(IC(2), BC(true), 4); qp_tile(IC(3), BC(true), 4);
  bf16x8 qf[12];
#pragma unroll
  for (int t = 0; t < 6; ++t) { qf[2 * t] = pack8n(acc[t], 0); qf[2 * t + 1] = pack8n(acc[t], 8); }
  if (br == 1 && w == 0 && THROTTLE_IT >= 0) poll_tags((gu64*)(wsb + WS_FA + idx * 64u), 0u, lane, tag);
  if (USE_PRIO) __builtin_amdgcn_s_setprio(3);

  const rsrc_t srdO = mk_rsrc((const char*)p.wo[br] + ((size_t)(768u * j) * DM + 160u * i) * 2, (u32)(HD * DM * 2) - (768u * j * DM + 160u * i) * 2u);
  u32 voffO[5];
#pragma unroll
  for (int v = 0; v < 5; ++v) { const u32 z = 64u * v + lane; voffO[v] = (z / 20u) * (DM * 2) + (z % 20u) * 16u; }
  auto wo_tile_of = [&](u32 n) { u32 t = n + 3u * i; return t >= 12u ? t - 12u : t; };
  auto wo_so = [&](u32 n) { return (wo_tile_of(n) * 64u + 16u * w) * (DM * 2); };
  auto wo_piece = [&](int n, u32 so, int v) { dma16<W_NT>(ldsb + ((n + 2) & 3) * WOS + w * 5120u + v * 1024u, voffO[v], srdO, so); };

  constexpr float CSC = 0.07216878364870322f * 1.4426950408889634f;
  constexpr float THR = 8.0f / CSC;
  f32x16 o[6];
#pragma unroll
  for (int t = 0; t < 6; ++t) o[t] = f32x16{};
  float m = -1e30f, l = 0.f;
  wait_bar<12>();
  u32 kb[4], vb[2];
#pragma unroll
  for (int q = 0; q < 4; ++q) kb[q] = baseK0 ^ (u32)(q << 5);
  vb[0] = baseV0; vb[1] = baseV0 ^ 64u;
  auto rdK = [&](int st) { return lds_b128(kb[st & 3] + (st >> 2) * 128); };
  auto rdV = [&](int n, int hi) { const int t = n % 6, hf = n / 6; return lds_tr(vb[t & 1] + (t >> 1) * 128 + hf * (16 * 384) + hi * (8 * 384)); };
  f32x16 s;
  auto qk = [&](const bf16x8 (&kf)[6]) {
    bf16x8 kg[6];
    s = f32x16{};
#pragma unroll
    for (int n = 0; n < 6; ++n) { MF(s, kf[n], qf[n]); kg[n] = rdK(n + 6); PIN(); }
#pragma unroll
    for (int n = 0; n < 6; ++n) { MF(s, kg[n], qf[n + 6]); PIN(); }
  };
  {
    bf16x8 kf[6];
#pragma unroll
    for (int n = 0; n < 6; ++n) kf[n] = rdK(n);
    PIN();
    qk(kf);
  }
  u32 slot = 0;
  auto att_iter = [&](auto MODEc, u32 it) {
    constexpr int MODE = decltype(MODEc)::value;
    if constexpr (MODE <= 1) wait_bar<6>(); else if constexpr (MODE == 2) wait_bar<3>(); else wait_bar<5>();
    PIN();
    s16x4 vlo[12], vhi[12];
#pragma unroll
    for (int n = 0; n < 6; ++n) { vlo[n] = rdV(n, 0); vhi[n] = rdV(n, 1); }
    const u32 nslot = slot == 2 * SLOT ? 0u : slot + SLOT, pslot = slot == 0 ? 2 * SLOT : slot - SLOT;
    const u32 dK = nslot - slot;
#pragma unroll
    for (int q = 0; q < 4; ++q) kb[q] += dK;
    if (br == 0 && it == THROTTLE_IT && tid == 0) put_tag((gu64*)(wsb + WS_FA + idx * 64u), tag);
    PIN();
    float pm = fmaxf(fmaxf(s[0], s[1]), s[2]);
#pragma unroll
    for (int k = 3; k < 15; k += 2) pm = fmaxf(fmaxf(pm, s[k]), s[k + 1]);
    pm = fmaxf(pm, s[15]);
    { auto rr = __builtin_amdgcn_permlane32_swap(__float_as_uint(pm), __float_as_uint(pm), false, false); pm = fmaxf(__uint_as_float(rr[0]), __uint_as_float(rr[1])); }
    if (__builtin_expect(__builtin_amdgcn_ballot_w64(pm > m + THR) != 0ull, 0)) {
      const float mn = fmaxf(m, pm), al = __builtin_amdgcn_exp2f((m - mn) * CSC);
      m = mn; l *= al;
#pragma unroll
      for (int t = 0; t < 6; ++t)
#pragma unroll
        for (int k = 0; k < 16; ++k) o[t][k] *= al;
    }
    const float mc = -m * CSC;
#pragma unroll
    for (int k = 0; k < 8; ++k) s[k] = __builtin_amdgcn_exp2f(fmaf(s[k], CSC, mc));
    const bf16x8 pf0 = pack8n(s, 0);
    PIN();
    u32 so14 = 0;
    if constexpr (MODE >= 2) so14 = wo_so(MODE - 2);
#pragma unroll
    for (int n = 0; n < 6; ++n) {
      MF(o[n], comb(vlo[n], vhi[n]), pf0);
      vlo[n + 6] = rdV(n + 6, 0); vhi[n + 6] = rdV(n + 6, 1);
      if (n < 2) { s[8 + 2 * n] = __builtin_amdgcn_exp2f(fmaf(s[8 + 2 * n], CSC, mc)); s[9 + 2 * n] = __builtin_amdgcn_exp2f(fmaf(s[9 + 2 * n], CSC, mc)); }
      else s[10 + n] = __builtin_amdgcn_exp2f(fmaf(s[10 + n], CSC, mc));
      if constexpr (MODE <= 1) { if ((n & 1) == 0) v_piece(it + 2, pslot, n >> 1); }
      else { if (n < 5) wo_piece(MODE - 2, so14, n); }
      PIN();
    }
    const bf16x8 pf1 = pack8n(s, 8);
    PIN();
    float ps = 0.f;
    bf16x8 kf[6];
#pragma unroll
    for (int n = 0; n < 6; ++n) {
      MF(o[n], comb(vlo[n + 6], vhi[n + 6]), pf1);
      if constexpr (MODE < 3) kf[n] = rdK(n);
      if (n < 5) { ps += s[3 * n]; ps += s[3 * n + 1]; ps += s[3 * n + 2]; } else { ps += s[15]; l += ps; }
      if constexpr (MODE == 0) { if ((n & 1) == 0) k_piece(it + 3, slot, n >> 1); }
      PIN();
    }
    asm volatile("" : "+v"(l));
    vb[0] += dK; vb[1] += dK;
    slot = nslot;
    if constexpr (MODE < 3) qk(kf);
  };
  for (u32 it = 0; it < 13; ++it) att_iter(IC(0), it);
  att_iter(IC(1), 13); att_iter(IC(2), 14); att_iter(IC(3), 15);
  if (USE_PRIO) __builtin_amdgcn_s_setprio(0);
  { auto rr = __builtin_amdgcn_permlane32_swap(__float_as_uint(l), __float_as_uint(l), false, false); l = __uint_as_float(rr[0]) + __uint_as_float(rr[1]); }
  const float inv = 1.0f / l;
  const rsrc_t srdOb = mk_rsrc(wsb + WS_O + (size_t)((br * NB + b) * NH) * 4 * 12288, NH * 4 * 12288);
  const u32 voffL = lane * 16u;
  u32x4 own[12];
  {
    const u32 so = (h * 4u + w) * 12288u;
#define ST_O(T) do { own[2 * T] = __builtin_bit_cast(u32x4, pack8(o[T], 0, inv)); own[2 * T + 1] = __builtin_bit_cast(u32x4, pack8(o[T], 8, inv)); \
      st16_sc1<(2 * T) * 1024 % 4096>(own[2 * T], voffL, srdOb, so + ((2 * T) * 1024 / 4096) * 4096); st16_sc1<(2 * T + 1) * 1024 % 4096>(own[2 * T + 1], voffL, srdOb, so + ((2 * T + 1) * 1024 / 4096) * 4096); } while (0)
    ST_O(0); ST_O(1); ST_O(2); ST_O(3); ST_O(4); ST_O(5);
#undef ST_O
  }

  f32x16 oa[5];
#pragma unroll
  for (int t = 0; t < 5; ++t) oa[t] = f32x16{};
  const u32 baseO = ldsb + (4u * hh + q4) * 320u + 32u * blkb + 16u * (pp & 1u) + 8u * (pp >> 1);
  auto rdO = [&](int n, int f, int hi) { return lds_tr(baseO + ((n + 2) & 3) * WOS + (f / 5) * (16 * 320) + (f % 5) * 64 + hi * (8 * 320)); };
  gu64* const FO = (gu64*)(wsb + WS_FO + (((br * NB + b) * 8u + j) * 4u + w) * 64u);
  u32x4 of[36];
  wait_bar<17>();
  s16x4 wlo[5], whi[5];
#pragma unroll
  for (int f = 0; f < 5; ++f) { wlo[f] = rdO(0, f, 0); whi[f] = rdO(0, f, 1); }
  { const u32 so = wo_so(2);
#pragma unroll
    for (int v = 0; v < 5; ++v) wo_piece(2, so, v); }
  PIN();
  auto op_tile = [&](auto Nc) {
    constexpr int N = decltype(Nc)::value;
    if constexpr (N == 3 || N == 6) {
      if constexpr (N == 3) { poll_tags(FO, 3u, lane, tag); asm volatile("" ::: "memory"); }
#pragma unroll
      for (int g = (N == 3 ? 0 : 2); g < (N == 3 ? 2 : 3); ++g) {
        const u32 so = ((4u * j + ((i + g + 1u) & 3u)) * 4u + w) * 12288u;
#pragma unroll
        for (int q = 0; q < 3; ++q) {
          ld16_sc1<0>(of[12 * g + 4 * q + 0], voffL, srdOb, so + q * 4096u); ld16_sc1<1024>(of[12 * g + 4 * q + 1], voffL, srdOb, so + q * 4096u);
          ld16_sc1<2048>(of[12 * g + 4 * q + 2], voffL, srdOb, so + q * 4096u); ld16_sc1<3072>(of[12 * g + 4 * q + 3], voffL, srdOb, so + q * 4096u);
        }
      }

    }
    if constexpr (N >= 3) {
      constexpr int B0 = 4 * (N - 3);
      constexpr int W = N == 3 ? 20 : N == 4 ? 21 : N == 5 ? 22 : 63;
      asm volatile("s_waitcnt vmcnt(%4)" : "+v"(of[B0]), "+v"(of[B0 + 1]), "+v"(of[B0 + 2]), "+v"(of[B0 + 3]) : "i"(W));
    }
    u32 so = 0;
#pragma unroll
    for (int f = 0; f < 20; ++f) {
      const u32x4 bfr = N < 3 ? own[4 * (N < 3 ? N : 0) + f / 5] : of[4 * (N >= 3 ? N - 3 : 0) + f / 5];
      MF(oa[f % 5], comb(wlo[f % 5], whi[f % 5]), __builtin_bit_cast(bf16x8, bfr));
      if (f == 15 && N < 11) {
        if constexpr (N == 0) wait_bar<17>(); else if constexpr (N == 1) wait_bar<5>(); else if constexpr (N == 2) wait_bar<5>();
        else if constexpr (N == 3 || N == 4) wait_bar<63>(); else if constexpr (N == 6 || N == 7) wait_bar<17>(); else if constexpr (N < 10) wait_bar<5>(); else wait_bar<0>();
        if constexpr (N == 1) { if (lane == 0) put_tag(FO + i, tag); }
        if constexpr (N + 3 < 12) so = wo_so(N + 3);
      }
      if (f < 15) { wlo[f % 5] = rdO(N, f + 5, 0); whi[f % 5] = rdO(N, f + 5, 1); }
      else if constexpr (N < 11) { wlo[f % 5] = rdO(N + 1, f - 15, 0); whi[f % 5] = rdO(N + 1, f - 15, 1); if constexpr (N + 3 < 12) wo_piece(N + 3, so, f - 15); }
      PIN();
    }
  };
  op_tile(IC(0)); op_tile(IC(1)); op_tile(IC(2)); op_tile(IC(3)); op_tile(IC(4)); op_tile(IC(5)); op_tile(IC(6)); op_tile(IC(7));
  op_tile(IC(8)); op_tile(IC(9)); op_tile(IC(10)); op_tile(IC(11));
  const rsrc_t srdP = mk_rsrc(wsb + WS_P + (size_t)((((br * NB + b) * 4u + i) * 4u + w) * 80u) * 1024u * PB, 80 * 1024 * PB);
#pragma unroll
  for (int u = 0; u < 10; ++u) st16_sc1<0>(__builtin_bit_cast(u32x4, pack8n(oa[u >> 1], (u & 1) * 8)), voffL, srdP, (u * 8u + j) * 1024u);
  asm volatile("s_waitcnt vmcnt(0)" ::: "memory");
  gu64* const FP = (gu64*)(wsb + WS_FP + (((br * NB + b) * 4u + i) * 4u + w) * 64u);
  if (lane == 0) put_tag(FP + j, tag);
  poll_tags(FP, 7u, lane, tag);
  asm volatile("" ::: "memory");
  for (u32 u = j; u < 10u; u += 8u) {
    float sum[8];
    u32x4 pv[8];
#pragma unroll
    for (int jj = 0; jj < 8; jj += 4) {
      ld16_sc1<0>(pv[jj], voffL, srdP, (u * 8u + jj) * 1024u); ld16_sc1<1024>(pv[jj + 1], voffL, srdP, (u * 8u + jj) * 1024u);
      ld16_sc1<2048>(pv[jj + 2], voffL, srdP, (u * 8u + jj) * 1024u); ld16_sc1<3072>(pv[jj + 3], voffL, srdP, (u * 8u + jj) * 1024u);
    }
    asm volatile("s_waitcnt vmcnt(0)" : "+v"(pv[0]), "+v"(pv[1]), "+v"(pv[2]), "+v"(pv[3]), "+v"(pv[4]), "+v"(pv[5]), "+v"(pv[6]), "+v"(pv[7]));
#pragma unroll
    for (int k = 0; k < 4; ++k) {
      float a = __uint_as_float(pv[0][k] << 16), c = __uint_as_float(pv[0][k] & 0xffff0000u);
#pragma unroll
      for (int jj = 1; jj < 8; ++jj) { a += __uint_as_float(pv[jj][k] << 16); c += __uint_as_float(pv[jj][k] & 0xffff0000u); }
      sum[2 * k] = a; sum[2 * k + 1] = c;
    }
    char* orow = (char*)p.out + ((size_t)(b * SQ + 32u * w + r) * (2 * DM) + br * DM + 160u * i + 16u * u + 8u * hh) * 2;
    u32x4 ov = {cvtpk(sum[0], sum[1]), cvtpk(sum[2], sum[3]), cvtpk(sum[4], sum[5]), cvtpk(sum[6], sum[7])};
    *(GAS u32x4*)orow = ov;
  }
  if (blk == 0 && w == 0) {
    u32 spins = 0;
    while (spins < (1u << 20)) {
      bool ok = true;
#pragma unroll
      for (int q = 0; q < 8; ++q) ok &= __hip_atomic_load((gu64*)(wsb + WS_ENTER) + q * 64 + lane, __ATOMIC_RELAXED, __HIP_MEMORY_SCOPE_AGENT) == tag;
      if (__builtin_amdgcn_ballot_w64(!ok) == 0ull) break;
      __builtin_amdgcn_s_sleep(8);
      if (++spins > (1u << 16)) break;
    }
    if (lane == 0) put_tag((gu64*)(wsb + WS_E), tag);
  }
}

extern "C" void kernel_launch(void* const* d_in, const int* in_sizes, int n_in, void* d_out, int out_size, void* d_ws, size_t ws_size, hipStream_t stream) {
  Params p;
  for (int br = 0; br < 2; ++br) {
    p.x[br] = d_in[br * 5 + 0]; p.kc[br] = d_in[br * 5 + 1]; p.vc[br] = d_in[br * 5 + 2]; p.wq[br] = d_in[br * 5 + 3]; p.wo[br] = d_in[br * 5 + 4];
  }
  p.out = d_out; p.ws = d_ws;
  hipLaunchKernelGGL(fused_kernel, dim3(512), dim3(256), 0, stream, p);
}
```

```cpp
#include <hip/hip_runtime.h>
#include <stdint.h>

#define ST_NT 0
typedef unsigned u32;
typedef unsigned long long u64;
typedef __bf16 bf16x8 __attribute__((ext_vector_type(8)));
typedef __bf16 bf16x2 __attribute__((ext_vector_type(2)));
typedef short s16x4 __attribute__((ext_vector_type(4)));
typedef short s16x8 __attribute__((ext_vector_type(8)));
typedef float f32x2 __attribute__((ext_vector_type(2)));
typedef float f32x16 __attribute__((ext_vector_type(16)));
typedef u32 u32x4 __attribute__((ext_vector_type(4)));
#define LAS __attribute__((address_space(3)))
#define GAS __attribute__((address_space(1)))
#define DI __device__ __forceinline__
typedef __amdgpu_buffer_rsrc_t rsrc_t;
typedef GAS u64 gu64;

constexpr int NB = 8, SQ = 128, DM = 640, NH = 32, SK = 512, DH = 192, HD = NH * DH;
constexpr u32 SLOT = 24576;
constexpr u32 WQS = 12288;
constexpr u32 XOFF = 49152;
constexpr u32 WOS = 20480;
constexpr u32 LDS_BYTES = 81920;
constexpr size_t WS_E = 0;
constexpr size_t WS_ENTER = 4096;
constexpr size_t WS_FA = 8192;
constexpr size_t WS_FO = 32768;
constexpr size_t WS_FP = 65536;
#define WS_BASE_MB 0
constexpr size_t WS_O = 131072 + (size_t)WS_BASE_MB * 1048576;
constexpr size_t WS_O_BYTES = (size_t)2 * NB * NH * 4 * 12288;
constexpr size_t WS_P = WS_O + WS_O_BYTES;
#define P_F32 0
constexpr u32 PB = P_F32 ? 2 : 1;
constexpr size_t WS_P_BYTES = (size_t)2 * NB * 4 * 4 * 10 * 8 * 1024 * PB;

struct Params {
  const void* x[2]; const void* kc[2]; const void* vc[2]; const void* wq[2]; const void* wo[2];
  void* out; void* ws;
};

DI u32 rfl(u32 v) { return __builtin_amdgcn_readfirstlane(v); }
DI rsrc_t mk_rsrc(const void* p, u32 bytes) { return __builtin_amdgcn_make_buffer_rsrc((void*)p, (short)0, (int)bytes, 0x00020000); }
DI u32 cvtpk(float lo, float hi) { f32x2 v = {lo, hi}; bf16x2 b = __builtin_convertvector(v, bf16x2); return __builtin_bit_cast(u32, b); }
DI bf16x8 pack8(const f32x16& a, int base, float sc) {
  u32x4 w = {cvtpk(a[base + 0] * sc, a[base + 1] * sc), cvtpk(a[base + 2] * sc, a[base + 3] * sc), cvtpk(a[base + 4] * sc, a[base + 5] * sc), cvtpk(a[base + 6] * sc, a[base + 7] * sc)};
  return __builtin_bit_cast(bf16x8, w);
}
DI bf16x8 pack8n(const f32x16& a, int base) {
  u32x4 w = {cvtpk(a[base + 0], a[base + 1]), cvtpk(a[base + 2], a[base + 3]), cvtpk(a[base + 4], a[base + 5]), cvtpk(a[base + 6], a[base + 7])};
  return __builtin_bit_cast(bf16x8, w);
}
#define MFMA(a, b, c) __builtin_amdgcn_mfma_f32_32x32x16_bf16((a), (b), (c), 0, 0, 0)
#define PIN() __builtin_amdgcn_sched_barrier(0)
#define MF(acc, a, b) do { acc = MFMA((a), (b), acc); asm volatile("" : "+v"(acc)); } while (0)

template <bool NT> DI void dma16(u32 lds, u32 voff, rsrc_t srd, u32 soff) {
  if constexpr (NT) asm volatile("s_mov_b32 m0, %0\n\ts_nop 0\n\tbuffer_load_dwordx4 %1, %2, %3 offen nt lds" :: "s"(lds), "v"(voff), "s"(srd), "s"(soff));
  else              asm volatile("s_mov_b32 m0, %0\n\ts_nop 0\n\tbuffer_load_dwordx4 %1, %2, %3 offen lds" :: "s"(lds), "v"(voff), "s"(srd), "s"(soff));
}
template <int IMM> DI void ld16_sc1(u32x4& dst, u32 voff, rsrc_t srd, u32 soff) {
  asm volatile("buffer_load_dwordx4 %0, %1, %2, %3 offen offset:%4 sc1" : "=v"(dst) : "v"(voff), "s"(srd), "s"(soff), "i"(IMM));
}
template <int IMM> DI void st16_sc1(const u32x4& src, u32 voff, rsrc_t srd, u32 soff) {
  asm volatile("buffer_store_dwordx4 %0, %1, %2, %3 offen offset:%4 sc1\n\ts_nop 1" :: "v"(src), "v"(voff), "s"(srd), "s"(soff), "i"(IMM) : "memory");
}
template <int N> DI void wait_bar() { asm volatile("s_waitcnt vmcnt(%0)\n\ts_barrier" :: "i"(N) : "memory"); }
template <int N> DI void wait_bar_lgkm() { asm volatile("s_waitcnt vmcnt(%0) lgkmcnt(0)\n\ts_barrier" :: "i"(N) : "memory"); }
DI bf16x8 lds_b128(u32 addr) { return *(const LAS bf16x8*)(size_t)addr; }
DI s16x4 lds_tr(u32 a) { return __builtin_amdgcn_ds_read_tr16_b64_v4i16((LAS s16x4*)(size_t)a); }
DI bf16x8 comb(s16x4 lo, s16x4 hi) { s16x8 v = __builtin_shufflevector(lo, hi, 0, 1, 2, 3, 4, 5, 6, 7); return __builtin_bit_cast(bf16x8, v); }
DI void put_tag(gu64* word, u64 tag) { __hip_atomic_store(word, tag, __ATOMIC_RELAXED, __HIP_MEMORY_SCOPE_AGENT); }
DI void poll_tags(gu64* words, u32 nmask, u32 lane, u64 tag) {
  u32 spins = 0;
  gu64* wp = words + (lane & nmask);
  while (true) {
    const u64 v = __hip_atomic_load(wp, __ATOMIC_RELAXED, __HIP_MEMORY_SCOPE_AGENT);
    if (__builtin_amdgcn_ballot_w64(v != tag) == 0ull) break;
    __builtin_amdgcn_s_sleep(2);
    if (++spins > (1u << 18)) break;
  }
}

#define KV_NT 1
#define W_NT 0
#define X_NT 0
#define THROTTLE_IT 12
#define USE_PRIO 1

__global__ __launch_bounds__(256, 2) void fused_kernel(Params p) {
  __shared__ __attribute__((aligned(1024))) char smem[LDS_BYTES];
  const u32 ldsb = (u32)(size_t)(LAS char*)smem;
  const u32 tid = threadIdx.x, lane = tid & 63u, w = rfl(tid >> 6);
  const u32 r = lane & 31u, hh = lane >> 5;
  const u32 blk = blockIdx.x, br = blk >> 8, idx = blk & 255u, j = idx & 7u, sl = idx >> 3, b = sl >> 2, i = sl & 3u, h = 4u * j + i;
  char* const wsb = (char*)p.ws;
  if (USE_PRIO && br == 0) __builtin_amdgcn_s_setprio(2);
  u64 ev;
  { const u32 vz = 0; asm volatile("global_load_dwordx2 %0, %1, %2 sc1" : "=v"(ev) : "v"(vz), "s"(wsb)); }

  const rsrc_t srdW = mk_rsrc((const char*)p.wq[br] + h * (DH * 2), (u32)(DM * HD * 2) - h * (DH * 2));
  const rsrc_t srdX = mk_rsrc((const char*)p.x[br] + (size_t)b * SQ * DM * 2, SQ * DM * 2);
  const rsrc_t srdK = mk_rsrc((const char*)p.kc[br] + (size_t)(b * NH + h) * SK * DH * 2, SK * DH * 2);
  const rsrc_t srdV = mk_rsrc((const char*)p.vc[br] + (size_t)(b * NH + h) * SK * DH * 2, SK * DH * 2);

  u32 voffW[3], voffK[3], voffV[3], voffX[2];
#pragma unroll
  for (int v = 0; v < 3; ++v) {
    const u32 z = 64u * v + lane, rr = z / 24u, ph = z % 24u;
    const u32 cV = ph ^ (((rr >> 1) & 1u) << 2);
    const u32 cK = (ph & ~7u) | ((ph & 7u) ^ (((w & 1u) << 2) | (rr >> 1)));
    voffW[v] = rr * (HD * 2) + cV * 16u;
    voffV[v] = rr * 384u + cV * 16u;
    voffK[v] = rr * 384u + cK * 16u;
  }
#pragma unroll
  for (int v = 0; v < 2; ++v) voffX[v] = (lane >> 3) * (DM * 2) + ((lane & 7u) ^ ((u32)(v << 2) | (lane >> 4))) * 16u;
  const u32 blkb = (lane >> 4) & 1u, q4 = (lane & 15u) >> 2, pp = lane & 3u, e = (q4 >> 1) & 1u;
  const u32 bW0 = ldsb + (8u * hh + q4) * 384u + (4u * e + 2u * blkb + (pp & 1u)) * 16u + 8u * (pp >> 1), bW1 = bW0 ^ 64u;
  const u32 baseV0 = ldsb + 12288u + (4u * hh + q4) * 384u + (4u * e + 2u * blkb + (pp >> 1)) * 16u + 8u * (pp & 1u);
  const u32 baseK0 = ldsb + r * 384u + ((hh ^ ((r >> 1) & 7u)) << 4);
  const u32 baseX0 = ldsb + XOFF + w * 4096u + r * 128u + ((hh ^ ((r >> 1) & 7u)) << 4);

  const u32 c0 = (b + (b >> 2) + 2u * i + (i >> 1)) % 10u;
  auto chunk_of = [&](u32 c) { u32 t = c + c0; return t >= 10u ? t - 10u : t; };
  auto wq_piece = [&](u32 slot, u32 so, int v) {
    dma16<W_NT>(ldsb + slot * WQS + w * 3072u + v * 1024u, voffW[v], srdW, so); };
  auto wq_so = [&](u32 t) { return (chunk_of(t >> 1) * 64u + (t & 1u) * 32u + 8u * w) * (HD * 2); };
  auto x_piece = [&](u32 par, u32 so, int v) {
    dma16<X_NT>(ldsb + XOFF + par * 16384u + w * 4096u + v * 1024u, voffX[v & 1], srdX, so + v * 8u * (DM * 2)); };
  auto x_so = [&](u32 c) { return 32u * w * (DM * 2) + chunk_of(c) * 128u; };
  auto k_piece = [&](u32 t, u32 slot_off, int v) { dma16<KV_NT>(ldsb + slot_off + w * 3072u + v * 1024u, voffK[v], srdK, t * 12288u + w * 3072u); };
  auto v_piece = [&](u32 t, u32 slot_off, int v) { dma16<KV_NT>(ldsb + slot_off + 12288u + w * 3072u + v * 1024u, voffV[v], srdV, t * 12288u + w * 3072u); };

  f32x16 acc[6];
#pragma unroll
  for (int t = 0; t < 6; ++t) acc[t] = f32x16{};
  {
    const u32 s0 = wq_so(0), s1 = wq_so(1), s2 = wq_so(2), s3 = wq_so(3), x0 = x_so(0), x1 = x_so(1);
#pragma unroll
    for (int v = 0; v < 3; ++v) wq_piece(0, s0, v);
#pragma unroll
    for (int v = 0; v < 4; ++v) x_piece(0, x0, v);
#pragma unroll
    for (int v = 0; v < 3; ++v) wq_piece(1, s1, v);
#pragma unroll
    for (int v = 0; v < 3; ++v) wq_piece(2, s2, v);
#pragma unroll
    for (int v = 0; v < 4; ++v) x_piece(1, x1, v);
#pragma unroll
    for (int v = 0; v < 3; ++v) wq_piece(3, s3, v);
  }
  asm volatile("s_waitcnt vmcnt(13)\n\ts_barrier" : "+v"(ev) :: "memory");
  const u64 tag = (((u64)rfl((u32)(ev >> 32)) << 32) | rfl((u32)ev)) + 1ull;
  if (tid == 0) put_tag((gu64*)(wsb + WS_ENTER) + blk, tag);
  auto rdA = [&](int slot, int ks, int m, int hi) {
    return lds_tr(((m & 1) ? bW1 : bW0) + slot * WQS + ks * (16 * 384) + (m >> 1) * 128 + hi * (4 * 384)); };
  auto rdX = [&](int par, int xks) { return lds_b128((baseX0 ^ (u32)(xks << 5)) + par * 16384); };
  s16x4 nlo[6], nhi[6];
  bf16x8 Xn;
  auto qp_ks0 = [&](auto Sc, const s16x4 (&lo)[6], const s16x4 (&hi)[6], const bf16x8& X) {
    constexpr int S = decltype(Sc)::value, PAR = S >> 1, XK = (S & 1) * 2;
    MF(acc[0], comb(lo[0], hi[0]), X); nlo[0] = rdA(S, 1, 0, 0); nhi[0] = rdA(S, 1, 0, 1); nlo[1] = rdA(S, 1, 1, 0); PIN();
    MF(acc[1], comb(lo[1], hi[1]), X); nhi[1] = rdA(S, 1, 1, 1); nlo[2] = rdA(S, 1, 2, 0); nhi[2] = rdA(S, 1, 2, 1); PIN();
    MF(acc[2], comb(lo[2], hi[2]), X); nlo[3] = rdA(S, 1, 3, 0); nhi[3] = rdA(S, 1, 3, 1); nlo[4] = rdA(S, 1, 4, 0); PIN();
    MF(acc[3], comb(lo[3], hi[3]), X); nhi[4] = rdA(S, 1, 4, 1); nlo[5] = rdA(S, 1, 5, 0); nhi[5] = rdA(S, 1, 5, 1); PIN();
    MF(acc[4], comb(lo[4], hi[4]), X); Xn = rdX(PAR, XK + 1); PIN();
    MF(acc[5], comb(lo[5], hi[5]), X); PIN();
  };
  {
    s16x4 Alo[6], Ahi[6];
#pragma unroll
    for (int m = 0; m < 6; ++m) { Alo[m] = rdA(0, 0, m, 0); Ahi[m] = rdA(0, 0, m, 1); }
    const bf16x8 X = rdX(0, 0);
    PIN();
    qp_ks0(std::integral_constant<int, 0>{}, Alo, Ahi, X);
  }
  auto qp_tile = [&](auto TTc, auto LASTc, u32 per) {
    constexpr int TT = decltype(TTc)::value; constexpr bool LAST = decltype(LASTc)::value;
    constexpr int PAR = TT >> 1;
    if constexpr (!LAST) { if constexpr (TT & 1) wait_bar_lgkm<6>(); else wait_bar_lgkm<10>(); }
    else { if constexpr (TT == 0) wait_bar_lgkm<10>(); else if constexpr (TT < 3) wait_bar_lgkm<6>(); else wait_bar_lgkm<63>(); }
    PIN();
    const u32 tn = 4u * per + TT + 4u;
    u32 sw = 0, sx = 0;
    if constexpr (!LAST) { sw = wq_so(tn); if constexpr (TT & 1) sx = x_so(tn >> 1); }
    constexpr int NS = (TT + 1) & 3, NPAR = NS >> 1, NXK = (NS & 1) * 2;
    constexpr bool MORE = !(LAST && TT == 3);
    s16x4 Alo[6], Ahi[6]; bf16x8 X;
#pragma unroll
    for (int m = 0; m < 6; ++m) {
      MF(acc[m], comb(nlo[m], nhi[m]), Xn);
      if constexpr (MORE) { Alo[m] = rdA(NS, 0, m, 0); Ahi[m] = rdA(NS, 0, m, 1); if (m == 0) X = rdX(NPAR, NXK); }
      if constexpr (!LAST) {
        if constexpr (TT & 1) {
          if (m == 0) { x_piece(PAR, sx, 0); } if (m == 1) { x_piece(PAR, sx, 1); x_piece(PAR, sx, 2); } if (m == 2) x_piece(PAR, sx, 3);
          if (m >= 3) wq_piece(TT, sw, m - 3);
        } else { if (m & 1) wq_piece(TT, sw, m >> 1); }
      } else {
        if constexpr (TT == 0) { if (m & 1) k_piece(0, 0, m >> 1); }
        if constexpr (TT == 1) { if (m & 1) v_piece(0, 0, m >> 1); }
        if constexpr (TT == 2) { if (m & 1) k_piece(1, SLOT, m >> 1); }
        if constexpr (TT == 3) { if (m < 3) v_piece(1, SLOT, m); else k_piece(2, 2 * SLOT, m - 3); }
      }
      PIN();
    }
    if constexpr (MORE) qp_ks0(std::integral_constant<int, NS>{}, Alo, Ahi, X);
  };
  using std::integral_constant;
#define IC(n) integral_constant<int, n>{}
#define BC(n) integral_constant<bool, n>{}
  for (u32 per = 0; per < 4; ++per) { qp_tile(IC(0), BC(false), per); qp_tile(IC(1), BC(false), per); qp_tile(IC(2), BC(false), per); qp_tile(IC(3), BC(false), per); }
  qp_tile(IC(0), BC(true), 4); qp_tile(IC(1), BC(true), 4); qp_tile(IC(2), BC(true), 4); qp_tile(IC(3), BC(true), 4);
  bf16x8 qf[12];
#pragma unroll
  for (int t = 0; t < 6; ++t) { qf[2 * t] = pack8n(acc[t], 0); qf[2 * t + 1] = pack8n(acc[t], 8); }
  if (br == 1 && w == 0 && THROTTLE_IT >= 0) poll_tags((gu64*)(wsb + WS_FA + idx * 64u), 0u, lane, tag);
  if (USE_PRIO) __builtin_amdgcn_s_setprio(3);

  const rsrc_t srdO = mk_rsrc((const char*)p.wo[br] + ((size_t)(768u * j) * DM + 160u * i) * 2, (u32)(HD * DM * 2) - (768u * j * DM + 160u * i) * 2u);
  u32 voffO[5];
#pragma unroll
  for (int v = 0; v < 5; ++v) { const u32 z = 64u * v + lane; voffO[v] = (z / 20u) * (DM * 2) + (z % 20u) * 16u; }
  auto wo_tile_of = [&](u32 n) { u32 t = n + 3u * i; return t >= 12u ? t - 12u : t; };
  auto wo_so = [&](u32 n) { return (wo_tile_of(n) * 64u + 16u * w) * (DM * 2); };
  auto wo_piece = [&](int n, u32 so, int v) { dma16<W_NT>(ldsb + ((n + 2) & 3) * WOS + w * 5120u + v * 1024u, voffO[v], srdO, so); };

  constexpr float CSC = 0.07216878364870322f * 1.4426950408889634f;
  constexpr float THR = 8.0f / CSC;
  f32x16 o[6];
#pragma unroll
  for (int t = 0; t < 6; ++t) o[t] = f32x16{};
  float m = -1e30f, l = 0.f;
  wait_bar<12>();
  u32 kb[4], vb[2];
#pragma unroll
  for (int q = 0; q < 4; ++q) kb[q] = baseK0 ^ (u32)(q << 5);
  vb[0] = baseV0; vb[1] = baseV0 ^ 64u;
  auto rdK = [&](int st) { return lds_b128(kb[st & 3] + (st >> 2) * 128); };
  auto rdV = [&](int n, int hi) { const int t = n % 6, hf = n / 6; return lds_tr(vb[t & 1] + (t >> 1) * 128 + hf * (16 * 384) + hi * (8 * 384)); };
  f32x16 s;
  auto qk = [&](const bf16x8 (&kf)[6]) {
    bf16x8 kg[6];
    s = f32x16{};
#pragma unroll
    for (int n = 0; n < 6; ++n) { MF(s, kf[n], qf[n]); kg[n] = rdK(n + 6); PIN(); }
#pragma unroll
    for (int n = 0; n < 6; ++n) { MF(s, kg[n], qf[n + 6]); PIN(); }
  };
  {
    bf16x8 kf[6];
#pragma unroll
    for (int n = 0; n < 6; ++n) kf[n] = rdK(n);
    PIN();
    qk(kf);
  }
  u32 slot = 0;
  auto att_iter = [&](auto MODEc, u32 it) {
    constexpr int MODE = decltype(MODEc)::value;
    if constexpr (MODE <= 1) wait_bar<6>(); else if constexpr (MODE == 2) wait_bar<3>(); else wait_bar<5>();
    PIN();
    s16x4 vlo[12], vhi[12];
#pragma unroll
    for (int n = 0; n < 6; ++n) { vlo[n] = rdV(n, 0); vhi[n] = rdV(n, 1); }
    const u32 nslot = slot == 2 * SLOT ? 0u : slot + SLOT, pslot = slot == 0 ? 2 * SLOT : slot - SLOT;
    const u32 dK = nslot - slot;
#pragma unroll
    for (int q = 0; q < 4; ++q) kb[q] += dK;
    if (br == 0 && it == THROTTLE_IT && tid == 0) put_tag((gu64*)(wsb + WS_FA + idx * 64u), tag);
    PIN();
    float pm = fmaxf(fmaxf(s[0], s[1]), s[2]);
#pragma unroll
    for (int k = 3; k < 15; k += 2) pm = fmaxf(fmaxf(pm, s[k]), s[k + 1]);
    pm = fmaxf(pm, s[15]);
    { auto rr = __builtin_amdgcn_permlane32_swap(__float_as_uint(pm), __float_as_uint(pm), false, false); pm = fmaxf(__uint_as_float(rr[0]), __uint_as_float(rr[1])); }
    if (__builtin_expect(__builtin_amdgcn_ballot_w64(pm > m + THR) != 0ull, 0)) {
      const float mn = fmaxf(m, pm), al = __builtin_amdgcn_exp2f((m - mn) * CSC);
      m = mn; l *= al;
#pragma unroll
      for (int t = 0; t < 6; ++t)
#pragma unroll
        for (int k = 0; k < 16; ++k) o[t][k] *= al;
    }
    const float mc = -m * CSC;
#pragma unroll
    for (int k = 0; k < 8; ++k) s[k] = __builtin_amdgcn_exp2f(fmaf(s[k], CSC, mc));
    const bf16x8 pf0 = pack8n(s, 0);
    PIN();
    u32 so14 = 0;
    if constexpr (MODE >= 2) so14 = wo_so(MODE - 2);
#pragma unroll
    for (int n = 0; n < 6; ++n) {
      MF(o[n], comb(vlo[n], vhi[n]), pf0);
      vlo[n + 6] = rdV(n + 6, 0); vhi[n + 6] = rdV(n + 6, 1);
      if (n < 2) { s[8 + 2 * n] = __builtin_amdgcn_exp2f(fmaf(s[8 + 2 * n], CSC, mc)); s[9 + 2 * n] = __builtin_amdgcn_exp2f(fmaf(s[9 + 2 * n], CSC, mc)); }
      else s[10 + n] = __builtin_amdgcn_exp2f(fmaf(s[10 + n], CSC, mc));
      if constexpr (MODE <= 1) { if ((n & 1) == 0) v_piece(it + 2, pslot, n >> 1); }
      else { if (n < 5) wo_piece(MODE - 2, so14, n); }
      PIN();
    }
    const bf16x8 pf1 = pack8n(s, 8);
    PIN();
    float ps = 0.f;
    bf16x8 kf[6];
#pragma unroll
    for (int n = 0; n < 6; ++n) {
      MF(o[n], comb(vlo[n + 6], vhi[n + 6]), pf1);
      if constexpr (MODE < 3) kf[n] = rdK(n);
      if (n < 5) { ps += s[3 * n]; ps += s[3 * n + 1]; ps += s[3 * n + 2]; } else { ps += s[15]; l += ps; }
      if constexpr (MODE == 0) { if ((n & 1) == 0) k_piece(it + 3, slot, n >> 1); }
      PIN();
    }
    asm volatile("" : "+v"(l));
    vb[0] += dK; vb[1] += dK;
    slot = nslot;
    if constexpr (MODE < 3) qk(kf);
  };
  for (u32 it = 0; it < 13; ++it) att_iter(IC(0), it);
  att_iter(IC(1), 13); att_iter(IC(2), 14); att_iter(IC(3), 15);
  if (USE_PRIO) __builtin_amdgcn_s_setprio(0);
  { auto rr = __builtin_amdgcn_permlane32_swap(__float_as_uint(l), __float_as_uint(l), false, false); l = __uint_as_float(rr[0]) + __uint_as_float(rr[1]); }
  const float inv = 1.0f / l;
  const rsrc_t srdOb = mk_rsrc(wsb + WS_O + (size_t)((br * NB + b) * NH) * 4 * 12288, NH * 4 * 12288);
  const u32 voffL = lane * 16u;
  u32x4 own[12];
  {
    const u32 so = (h * 4u + w) * 12288u;
#define ST_O(T) do { own[2 * T] = __builtin_bit_cast(u32x4, pack8(o[T], 0, inv)); own[2 * T + 1] = __builtin_bit_cast(u32x4, pack8(o[T], 8, inv)); \
      st16_sc1<(2 * T) * 1024 % 4096>(own[2 * T], voffL, srdOb, so + ((2 * T) * 1024 / 4096) * 4096); st16_sc1<(2 * T + 1) * 1024 % 4096>(own[2 * T + 1], voffL, srdOb, so + ((2 * T + 1) * 1024 / 4096) * 4096); } while (0)
    ST_O(0); ST_O(1); ST_O(2); ST_O(3); ST_O(4); ST_O(5);
#undef ST_O
  }

  f32x16 oa[5];
#pragma unroll
  for (int t = 0; t < 5; ++t) oa[t] = f32x16{};
  const u32 baseO = ldsb + (4u * hh + q4) * 320u + 32u * blkb + 16u * (pp & 1u) + 8u * (pp >> 1);
  auto rdO = [&](int n, int f, int hi) { return lds_tr(baseO + ((n + 2) & 3) * WOS + (f / 5) * (16 * 320) + (f % 5) * 64 + hi * (8 * 320)); };
  gu64* const FO = (gu64*)(wsb + WS_FO + (((br * NB + b) * 8u + j) * 4u + w) * 64u);
  u32x4 of[36];
  wait_bar<17>();
  s16x4 wlo[5], whi[5];
#pragma unroll
  for (int f = 0; f < 5; ++f) { wlo[f] = rdO(0, f, 0); whi[f] = rdO(0, f, 1); }
  { const u32 so = wo_so(2);
#pragma unroll
    for (int v = 0; v < 5; ++v) wo_piece(2, so, v); }
  PIN();
  auto op_tile = [&](auto Nc) {
    constexpr int N = decltype(Nc)::value;
    if constexpr (N == 3 || N == 6) {
      if constexpr (N == 3) { poll_tags(FO, 3u, lane, tag); asm volatile("" ::: "memory"); }
#pragma unroll
      for (int g = (N == 3 ? 0 : 2); g < (N == 3 ? 2 : 3); ++g) {
        const u32 so = ((4u * j + ((i + g + 1u) & 3u)) * 4u + w) * 12288u;
#pragma unroll
        for (int q = 0; q < 3; ++q) {
          ld16_sc1<0>(of[12 * g + 4 * q + 0], voffL, srdOb, so + q * 4096u); ld16_sc1<1024>(of[12 * g + 4 * q + 1], voffL, srdOb, so + q * 4096u);
          ld16_sc1<2048>(of[12 * g + 4 * q + 2], voffL, srdOb, so + q * 4096u); ld16_sc1<3072>(of[12 * g + 4 * q + 3], voffL, srdOb, so + q * 4096u);
        }
      }

    }
    if constexpr (N >= 3) {
      constexpr int B0 = 4 * (N - 3);
      constexpr int W = N == 3 ? 20 : N == 4 ? 21 : N == 5 ? 22 : 63;
      asm volatile("s_waitcnt vmcnt(%4)" : "+v"(of[B0]), "+v"(of[B0 + 1]), "+v"(of[B0 + 2]), "+v"(of[B0 + 3]) : "i"(W));
    }
    u32 so = 0;
#pragma unroll
    for (int f = 0; f < 20; ++f) {
      const u32x4 bfr = N < 3 ? own[4 * (N < 3 ? N : 0) + f / 5] : of[4 * (N >= 3 ? N - 3 : 0) + f / 5];
      MF(oa[f % 5], comb(wlo[f % 5], whi[f % 5]), __builtin_bit_cast(bf16x8, bfr));
      if (f == 15 && N < 11) {
        if constexpr (N == 0) wait_bar<17>(); else if constexpr (N == 1) wait_bar<5>(); else if constexpr (N == 2) wait_bar<5>();
        else if constexpr (N == 3 || N == 4) wait_bar<63>(); else if constexpr (N == 6 || N == 7) wait_bar<17>(); else if constexpr (N < 10) wait_bar<5>(); else wait_bar<0>();
        if constexpr (N == 1) { if (lane == 0) put_tag(FO + i, tag); }
        if constexpr (N + 3 < 12) so = wo_so(N + 3);
      }
      if (f < 15) { wlo[f % 5] = rdO(N, f + 5, 0); whi[f % 5] = rdO(N, f + 5, 1); }
      else if constexpr (N < 11) { wlo[f % 5] = rdO(N + 1, f - 15, 0); whi[f % 5] = rdO(N + 1, f - 15, 1); if constexpr (N + 3 < 12) wo_piece(N + 3, so, f - 15); }
      PIN();
    }
  };
  op_tile(IC(0)); op_tile(IC(1)); op_tile(IC(2)); op_tile(IC(3)); op_tile(IC(4)); op_tile(IC(5)); op_tile(IC(6)); op_tile(IC(7));
  op_tile(IC(8)); op_tile(IC(9)); op_tile(IC(10)); op_tile(IC(11));
  const rsrc_t srdP = mk_rsrc(wsb + WS_P + (size_t)((((br * NB + b) * 4u + i) * 4u + w) * 80u) * 1024u * PB, 80 * 1024 * PB);
#pragma unroll
  for (int u = 0; u < 10; ++u) st16_sc1<0>(__builtin_bit_cast(u32x4, pack8n(oa[u >> 1], (u & 1) * 8)), voffL, srdP, (u * 8u + j) * 1024u);
  asm volatile("s_waitcnt vmcnt(0)" ::: "memory");
  gu64* const FP = (gu64*)(wsb + WS_FP + (((br * NB + b) * 4u + i) * 4u + w) * 64u);
  if (lane == 0) put_tag(FP + j, tag);
  poll_tags(FP, 7u, lane, tag);
  asm volatile("" ::: "memory");
  for (u32 u = j; u < 10u; u += 8u) {
    float sum[8];
    u32x4 pv[8];
#pragma unroll
    for (int jj = 0; jj < 8; jj += 4) {
      ld16_sc1<0>(pv[jj], voffL, srdP, (u * 8u + jj) * 1024u); ld16_sc1<1024>(pv[jj + 1], voffL, srdP, (u * 8u + jj) * 1024u);
      ld16_sc1<2048>(pv[jj + 2], voffL, srdP, (u * 8u + jj) * 1024u); ld16_sc1<3072>(pv[jj + 3], voffL, srdP, (u * 8u + jj) * 1024u);
    }
    asm volatile("s_waitcnt vmcnt(0)" : "+v"(pv[0]), "+v"(pv[1]), "+v"(pv[2]), "+v"(pv[3]), "+v"(pv[4]), "+v"(pv[5]), "+v"(pv[6]), "+v"(pv[7]));
#pragma unroll
    for (int k = 0; k < 4; ++k) {
      float a = __uint_as_float(pv[0][k] << 16), c = __uint_as_float(pv[0][k] & 0xffff0000u);
#pragma unroll
      for (int jj = 1; jj < 8; ++jj) { a += __uint_as_float(pv[jj][k] << 16); c += __uint_as_float(pv[jj][k] & 0xffff0000u); }
      sum[2 * k] = a; sum[2 * k + 1] = c;
    }
    char* orow = (char*)p.out + ((size_t)(b * SQ + 32u * w + r) * (2 * DM) + br * DM + 160u * i + 16u * u + 8u * hh) * 2;
    u32x4 ov = {cvtpk(sum[0], sum[1]), cvtpk(sum[2], sum[3]), cvtpk(sum[4], sum[5]), cvtpk(sum[6], sum[7])};
    *(GAS u32x4*)orow = ov;
  }
  if (blk == 0 && w == 0) {
    u32 spins = 0;
    while (spins < (1u << 20)) {
      bool ok = true;
#pragma unroll
      for (int q = 0; q < 8; ++q) ok &= __hip_atomic_load((gu64*)(wsb + WS_ENTER) + q * 64 + lane, __ATOMIC_RELAXED, __HIP_MEMORY_SCOPE_AGENT) == tag;
      if (__builtin_amdgcn_ballot_w64(!ok) == 0ull) break;
      __builtin_amdgcn_s_sleep(8);
      if (++spins > (1u << 16)) break;
    }
    if (lane == 0) put_tag((gu64*)(wsb + WS_E), tag);
  }
}

extern "C" void kernel_launch(void* const* d_in, const int* in_sizes, int n_in, void* d_out, int out_size, void* d_ws, size_t ws_size, hipStream_t stream) {
  Params p;
  for (int br = 0; br < 2; ++br) {
    p.x[br] = d_in[br * 5 + 0]; p.kc[br] = d_in[br * 5 + 1]; p.vc[br] = d_in[br * 5 + 2]; p.wq[br] = d_in[br * 5 + 3]; p.wo[br] = d_in[br * 5 + 4];
  }
  p.out = d_out; p.ws = d_ws;
  hipLaunchKernelGGL(fused_kernel, dim3(512), dim3(256), 0, stream, p);
}
```

```cpp
#include <hip/hip_runtime.h>
#include <stdint.h>

#define ST_NT 0
typedef unsigned u32;
typedef unsigned long long u64;
typedef __bf16 bf16x8 __attribute__((ext_vector_type(8)));
typedef __bf16 bf16x2 __attribute__((ext_vector_type(2)));
typedef short s16x4 __attribute__((ext_vector_type(4)));
typedef short s16x8 __attribute__((ext_vector_type(8)));
typedef float f32x2 __attribute__((ext_vector_type(2)));
typedef float f32x16 __attribute__((ext_vector_type(16)));
typedef u32 u32x4 __attribute__((ext_vector_type(4)));
#define LAS __attribute__((address_space(3)))
#define GAS __attribute__((address_space(1)))
#define DI __device__ __forceinline__
typedef __amdgpu_buffer_rsrc_t rsrc_t;
typedef GAS u64 gu64;

constexpr int NB = 8, SQ = 128, DM = 640, NH = 32, SK = 512, DH = 192, HD = NH * DH;
constexpr u32 SLOT = 24576;
constexpr u32 WQS = 12288;
constexpr u32 XOFF = 49152;
constexpr u32 WOS = 20480;
constexpr u32 LDS_BYTES = 81920;
constexpr size_t WS_E = 0;
constexpr size_t WS_ENTER = 4096;
constexpr size_t WS_FA = 8192;
constexpr size_t WS_FO = 32768;
constexpr size_t WS_FP = 65536;
#define WS_BASE_MB 208
constexpr size_t WS_O = 131072 + (size_t)WS_BASE_MB * 1048576;
constexpr size_t WS_O_BYTES = (size_t)2 * NB * NH * 4 * 12288;
constexpr size_t WS_P = WS_O + WS_O_BYTES;
#define P_F32 0
constexpr u32 PB = P_F32 ? 2 : 1;
constexpr size_t WS_P_BYTES = (size_t)2 * NB * 4 * 4 * 10 * 8 * 1024 * PB;

struct Params {
  const void* x[2]; const void* kc[2]; const void* vc[2]; const void* wq[2]; const void* wo[2];
  void* out; void* ws;
};

DI u32 rfl(u32 v) { return __builtin_amdgcn_readfirstlane(v); }
DI rsrc_t mk_rsrc(const void* p, u32 bytes) { return __builtin_amdgcn_make_buffer_rsrc((void*)p, (short)0, (int)bytes, 0x00020000); }
DI u32 cvtpk(float lo, float hi) { f32x2 v = {lo, hi}; bf16x2 b = __builtin_convertvector(v, bf16x2); return __builtin_bit_cast(u32, b); }
DI bf16x8 pack8(const f32x16& a, int base, float sc) {
  u32x4 w = {cvtpk(a[base + 0] * sc, a[base + 1] * sc), cvtpk(a[base + 2] * sc, a[base + 3] * sc), cvtpk(a[base + 4] * sc, a[base + 5] * sc), cvtpk(a[base + 6] * sc, a[base + 7] * sc)};
  return __builtin_bit_cast(bf16x8, w);
}
DI bf16x8 pack8n(const f32x16& a, int base) {
  u32x4 w = {cvtpk(a[base + 0], a[base + 1]), cvtpk(a[base + 2], a[base + 3]), cvtpk(a[base + 4], a[base + 5]), cvtpk(a[base + 6], a[base + 7])};
  return __builtin_bit_cast(bf16x8, w);
}
#define MFMA(a, b, c) __builtin_amdgcn_mfma_f32_32x32x16_bf16((a), (b), (c), 0, 0, 0)
#define PIN() __builtin_amdgcn_sched_barrier(0)
#define MF(acc, a, b) do { acc = MFMA((a), (b), acc); asm volatile("" : "+v"(acc)); } while (0)

template <bool NT> DI void dma16(u32 lds, u32 voff, rsrc_t srd, u32 soff) {
  if constexpr (NT) asm volatile("s_mov_b32 m0, %0\n\ts_nop 0\n\tbuffer_load_dwordx4 %1, %2, %3 offen nt lds" :: "s"(lds), "v"(voff), "s"(srd), "s"(soff));
  else              asm volatile("s_mov_b32 m0, %0\n\ts_nop 0\n\tbuffer_load_dwordx4 %1, %2, %3 offen lds" :: "s"(lds), "v"(voff), "s"(srd), "s"(soff));
}
template <bool NT> DI void pf4(u32 lds, u32 voff, rsrc_t srd) {
  if constexpr (NT) asm volatile("s_mov_b32 m0, %0\n\ts_nop 0\n\tbuffer_load_dword %1, %2, 0 offen nt lds" :: "s"(lds), "v"(voff), "s"(srd));
  else              asm volatile("s_mov_b32 m0, %0\n\ts_nop 0\n\tbuffer_load_dword %1, %2, 0 offen lds" :: "s"(lds), "v"(voff), "s"(srd));
}
DI void set_m0(u32 base, int imm) { asm volatile("s_add_u32 m0, %0, %1" :: "s"(base), "i"(imm) : "scc"); }
template <bool NT> DI void dma_go(u32 voff, rsrc_t srd, u32 soff) {
  if constexpr (NT) asm volatile("buffer_load_dwordx4 %0, %1, %2 offen nt lds" :: "v"(voff), "s"(srd), "s"(soff));
  else              asm volatile("buffer_load_dwordx4 %0, %1, %2 offen lds" :: "v"(voff), "s"(srd), "s"(soff));
}
template <int N> DI void wait_lgkm() { __builtin_amdgcn_s_waitcnt(0xC07F | (N << 8)); }
DI float max3(float a, float b, float c) { return __builtin_elementwise_maximum(__builtin_elementwise_maximum(a, b), c); }
template <int IMM> DI void ld16_sc1(u32x4& dst, u32 voff, rsrc_t srd, u32 soff) {
  asm volatile("buffer_load_dwordx4 %0, %1, %2, %3 offen offset:%4 sc1" : "=v"(dst) : "v"(voff), "s"(srd), "s"(soff), "i"(IMM));
}
template <int IMM> DI void st16_sc1(const u32x4& src, u32 voff, rsrc_t srd, u32 soff) {
  asm volatile("buffer_store_dwordx4 %0, %1, %2, %3 offen offset:%4 sc1\n\ts_nop 1" :: "v"(src), "v"(voff), "s"(srd), "s"(soff), "i"(IMM) : "memory");
}
template <int N> DI void wait_bar() { asm volatile("s_waitcnt vmcnt(%0)\n\ts_barrier" :: "i"(N) : "memory"); }
template <int N> DI void wait_bar_lgkm() {
  asm volatile("" ::: "memory");
  __builtin_amdgcn_s_waitcnt(0x0070 | (N & 15) | ((N >> 4) << 14));
  asm volatile("s_barrier" ::: "memory");
}
DI bf16x8 lds_b128(u32 addr) { return *(const LAS bf16x8*)(size_t)addr; }
DI s16x4 lds_tr(u32 a) { return __builtin_amdgcn_ds_read_tr16_b64_v4i16((LAS s16x4*)(size_t)a); }
DI bf16x8 comb(s16x4 lo, s16x4 hi) { s16x8 v = __builtin_shufflevector(lo, hi, 0, 1, 2, 3, 4, 5, 6, 7); return __builtin_bit_cast(bf16x8, v); }
DI void put_tag(gu64* word, u64 tag) { __hip_atomic_store(word, tag, __ATOMIC_RELAXED, __HIP_MEMORY_SCOPE_AGENT); }
DI void poll_tags(gu64* words, u32 nmask, u32 lane, u64 tag) {
  u32 spins = 0;
  gu64* wp = words + (lane & nmask);
  while (true) {
    const u64 v = __hip_atomic_load(wp, __ATOMIC_RELAXED, __HIP_MEMORY_SCOPE_AGENT);
    if (__builtin_amdgcn_ballot_w64(v != tag) == 0ull) break;
    __builtin_amdgcn_s_sleep(2);
    if (++spins > (1u << 18)) break;
  }
}

#define KV_NT 1
#define W_NT 0
#define X_NT 0
#define THROTTLE_IT 12
#define USE_PRIO 1
#define B_DELAY_PER -1
#define WO_PF 1

__global__ __launch_bounds__(256, 2) void fused_kernel(Params p) {
  __shared__ __attribute__((aligned(1024))) char smem[LDS_BYTES];
  const u32 ldsb = (u32)(size_t)(LAS char*)smem;
  const u32 tid = threadIdx.x, lane = tid & 63u, w = rfl(tid >> 6);
  const u32 r = lane & 31u, hh = lane >> 5;
  const u32 blk = blockIdx.x, br = blk >> 8, idx = blk & 255u, j = idx & 7u, sl = idx >> 3, b = sl >> 2, i = sl & 3u, h = 4u * j + i;
  char* const wsb = (char*)p.ws;
  if (USE_PRIO && br == 0) __builtin_amdgcn_s_setprio(2);
  u64 ev;
  { const u32 vz = 0; asm volatile("global_load_dwordx2 %0, %1, %2 sc1" : "=v"(ev) : "v"(vz), "s"(wsb)); }

  const rsrc_t srdW = mk_rsrc((const char*)p.wq[br] + h * (DH * 2), (u32)(DM * HD * 2) - h * (DH * 2));
  const rsrc_t srdX = mk_rsrc((const char*)p.x[br] + (size_t)b * SQ * DM * 2, SQ * DM * 2);
  const rsrc_t srdK = mk_rsrc((const char*)p.kc[br] + (size_t)(b * NH + h) * SK * DH * 2, SK * DH * 2);
  const rsrc_t srdV = mk_rsrc((const char*)p.vc[br] + (size_t)(b * NH + h) * SK * DH * 2, SK * DH * 2);

  u32 voffW[3], voffK[3], voffV[3], voffX[2];
#pragma unroll
  for (int v = 0; v < 3; ++v) {
    const u32 z = 64u * v + lane, rr = z / 24u, ph = z % 24u;
    const u32 cV = ph ^ (((rr >> 1) & 1u) << 2);
    const u32 cK = (ph & ~7u) | ((ph & 7u) ^ (((w & 1u) << 2) | (rr >> 1)));
    voffW[v] = rr * (HD * 2) + cV * 16u;
    voffV[v] = rr * 384u + cV * 16u;
    voffK[v] = rr * 384u + cK * 16u;
  }
#pragma unroll
  for (int v = 0; v < 2; ++v) voffX[v] = (lane >> 3) * (DM * 2) + ((lane & 7u) ^ ((u32)(v << 2) | (lane >> 4))) * 16u;
  const u32 blkb = (lane >> 4) & 1u, q4 = (lane & 15u) >> 2, pp = lane & 3u, e = (q4 >> 1) & 1u;
  const u32 bW0 = ldsb + (8u * hh + q4) * 384u + (4u * e + 2u * blkb + (pp & 1u)) * 16u + 8u * (pp >> 1), bW1 = bW0 ^ 64u;
  const u32 baseV0 = ldsb + 12288u + (4u * hh + q4) * 384u + (4u * e + 2u * blkb + (pp >> 1)) * 16u + 8u * (pp & 1u);
  const u32 baseK0 = ldsb + r * 384u + ((hh ^ ((r >> 1) & 7u)) << 4);
  const u32 baseX0 = ldsb + XOFF + w * 4096u + r * 128u + ((hh ^ ((r >> 1) & 7u)) << 4);

  const u32 c0 = (b + (b >> 2) + 2u * i + (i >> 1)) % 10u;
  auto chunk_of = [&](u32 c) { u32 t = c + c0; return t >= 10u ? t - 10u : t; };
  auto wq_piece = [&](u32 slot, u32 so, int v) {
    dma16<W_NT>(ldsb + slot * WQS + w * 3072u + v * 1024u, voffW[v], srdW, so); };
  auto wq_so = [&](u32 t) { return (chunk_of(t >> 1) * 64u + (t & 1u) * 32u + 8u * w) * (HD * 2); };
  auto x_piece = [&](u32 par, u32 so, int v) {
    dma16<X_NT>(ldsb + XOFF + par * 16384u + w * 4096u + v * 1024u, voffX[v & 1], srdX, so + v * 8u * (DM * 2)); };
  auto x_so = [&](u32 c) { return 32u * w * (DM * 2) + chunk_of(c) * 128u; };
  auto k_piece = [&](u32 t, u32 slot_off, int v) { dma16<KV_NT>(ldsb + slot_off + w * 3072u + v * 1024u, voffK[v], srdK, t * 12288u + w * 3072u); };
  auto v_piece = [&](u32 t, u32 slot_off, int v) { dma16<KV_NT>(ldsb + slot_off + 12288u + w * 3072u + v * 1024u, voffV[v], srdV, t * 12288u + w * 3072u); };

  f32x16 acc[6];
#pragma unroll
  for (int t = 0; t < 6; ++t) acc[t] = f32x16{};
  {
    const u32 s0 = wq_so(0), s1 = wq_so(1), s2 = wq_so(2), s3 = wq_so(3), x0 = x_so(0), x1 = x_so(1);
#pragma unroll
    for (int v = 0; v < 3; ++v) wq_piece(0, s0, v);
#pragma unroll
    for (int v = 0; v < 4; ++v) x_piece(0, x0, v);
#pragma unroll
    for (int v = 0; v < 3; ++v) wq_piece(1, s1, v);
#pragma unroll
    for (int v = 0; v < 3; ++v) wq_piece(2, s2, v);
#pragma unroll
    for (int v = 0; v < 4; ++v) x_piece(1, x1, v);
#pragma unroll
    for (int v = 0; v < 3; ++v) wq_piece(3, s3, v);
  }
  asm volatile("s_waitcnt vmcnt(20)" : "+v"(ev) :: "memory");
  const u64 tag = (((u64)rfl((u32)(ev >> 32)) << 32) | rfl((u32)ev)) + 1ull;
  if (B_DELAY_PER >= 0 && br == 1 && w == 0) poll_tags((gu64*)(wsb + WS_FA + idx * 64u + 8u), 0u, lane, tag);
  asm volatile("s_waitcnt vmcnt(13)\n\ts_barrier" ::: "memory");
  if (tid == 0) put_tag((gu64*)(wsb + WS_ENTER) + blk, tag);
  auto rdA = [&](int slot, int ks, int m, int hi) {
    return lds_tr(((m & 1) ? bW1 : bW0) + slot * WQS + ks * (16 * 384) + (m >> 1) * 128 + hi * (4 * 384)); };
  auto rdX = [&](int par, int xks) { return lds_b128((baseX0 ^ (u32)(xks << 5)) + par * 16384); };
  s16x4 nlo[6], nhi[6];
  bf16x8 Xn;
  auto qp_ks0 = [&](auto Sc, const s16x4 (&lo)[6], const s16x4 (&hi)[6], const bf16x8& X) {
    constexpr int S = decltype(Sc)::value, PAR = S >> 1, XK = (S & 1) * 2;
    MF(acc[0], comb(lo[0], hi[0]), X); nlo[0] = rdA(S, 1, 0, 0); nhi[0] = rdA(S, 1, 0, 1); nlo[1] = rdA(S, 1, 1, 0); PIN();
    MF(acc[1], comb(lo[1], hi[1]), X); nhi[1] = rdA(S, 1, 1, 1); nlo[2] = rdA(S, 1, 2, 0); nhi[2] = rdA(S, 1, 2, 1); PIN();
    MF(acc[2], comb(lo[2], hi[2]), X); nlo[3] = rdA(S, 1, 3, 0); nhi[3] = rdA(S, 1, 3, 1); nlo[4] = rdA(S, 1, 4, 0); PIN();
    MF(acc[3], comb(lo[3], hi[3]), X); nhi[4] = rdA(S, 1, 4, 1); nlo[5] = rdA(S, 1, 5, 0); nhi[5] = rdA(S, 1, 5, 1); PIN();
    MF(acc[4], comb(lo[4], hi[4]), X); Xn = rdX(PAR, XK + 1); PIN();
    MF(acc[5], comb(lo[5], hi[5]), X); PIN();
  };
  {
    s16x4 Alo[6], Ahi[6];
#pragma unroll
    for (int m = 0; m < 6; ++m) { Alo[m] = rdA(0, 0, m, 0); Ahi[m] = rdA(0, 0, m, 1); }
    const bf16x8 X = rdX(0, 0);
    PIN();
    qp_ks0(std::integral_constant<int, 0>{}, Alo, Ahi, X);
  }
  auto qp_tile = [&](auto TTc, auto LASTc, u32 per) {
    constexpr int TT = decltype(TTc)::value; constexpr bool LAST = decltype(LASTc)::value;
    constexpr int PAR = TT >> 1;
    if constexpr (!LAST) { if constexpr (TT & 1) wait_bar_lgkm<6>(); else wait_bar_lgkm<10>(); }
    else { if constexpr (TT == 0) wait_bar_lgkm<10>(); else if constexpr (TT < 3) wait_bar_lgkm<6>(); else wait_bar_lgkm<63>(); }
    PIN();
    if constexpr (TT == 0 && B_DELAY_PER >= 0) { if (br == 0 && per == (u32)B_DELAY_PER && tid == 0) put_tag((gu64*)(wsb + WS_FA + idx * 64u + 8u), tag); }
    const u32 tn = 4u * per + TT + 4u;
    u32 sw = 0, sx = 0;
    if constexpr (!LAST) { sw = wq_so(tn); if constexpr (TT & 1) sx = x_so(tn >> 1); }
    constexpr int NS = (TT + 1) & 3, NPAR = NS >> 1, NXK = (NS & 1) * 2;
    constexpr bool MORE = !(LAST && TT == 3);
    s16x4 Alo[6], Ahi[6]; bf16x8 X;
#pragma unroll
    for (int m = 0; m < 6; ++m) {
      MF(acc[m], comb(nlo[m], nhi[m]), Xn);
      if constexpr (MORE) { Alo[m] = rdA(NS, 0, m, 0); Ahi[m] = rdA(NS, 0, m, 1); if (m == 0) X = rdX(NPAR, NXK); }
      if constexpr (!LAST) {
        if constexpr (TT & 1) {
          if (m == 0) { x_piece(PAR, sx, 0); } if (m == 1) { x_piece(PAR, sx, 1); x_piece(PAR, sx, 2); } if (m == 2) x_piece(PAR, sx, 3);
          if (m >= 3) wq_piece(TT, sw, m - 3);
        } else { if (m & 1) wq_piece(TT, sw, m >> 1); }
      } else {
        if constexpr (TT == 0) { if (m & 1) k_piece(0, 0, m >> 1); }
        if constexpr (TT == 1) { if (m & 1) v_piece(0, 0, m >> 1); }
        if constexpr (TT == 2) { if (m < 3) k_piece(1, SLOT, m); else k_piece(2, 2 * SLOT, m - 3); }
        if constexpr (TT == 3) { if (m & 1) v_piece(1, SLOT, m >> 1); }
      }
      PIN();
    }
    if constexpr (MORE) qp_ks0(std::integral_constant<int, NS>{}, Alo, Ahi, X);
  };
  using std::integral_constant;
#define IC(n) integral_constant<int, n>{}
#define BC(n) integral_constant<bool, n>{}
  for (u32 per = 0; per < 4; ++per) { qp_tile(IC(0), BC(false), per); qp_tile(IC(1), BC(false), per); qp_tile(IC(2), BC(false), per); qp_tile(IC(3), BC(false), per); }
  qp_tile(IC(0), BC(true), 4); qp_tile(IC(1), BC(true), 4); qp_tile(IC(2), BC(true), 4); qp_tile(IC(3), BC(true), 4);
  constexpr float CSC = 0.07216878364870322f * 1.4426950408889634f;
  bf16x8 qf[12];
#pragma unroll
  for (int t = 0; t < 6; ++t) { qf[2 * t] = pack8(acc[t], 0, CSC); qf[2 * t + 1] = pack8(acc[t], 8, CSC); }
  auto wo_pf = [&](u32 brr, u32 ldsdst) {
    pf4<false>(ldsdst + w * 256u, (240u * sl + tid) * 128u, mk_rsrc((const char*)p.wo[brr] + (size_t)(768u * j) * DM * 2, 768 * DM * 2));
  };
  if (WO_PF & 1) wo_pf(br, ldsb + 3 * SLOT);
  if (br == 1 && w == 0 && THROTTLE_IT >= 0) poll_tags((gu64*)(wsb + WS_FA + idx * 64u), 0u, lane, tag);
  if (USE_PRIO) __builtin_amdgcn_s_setprio(3);

  const rsrc_t srdO = mk_rsrc((const char*)p.wo[br] + ((size_t)(768u * j) * DM + 160u * i) * 2, (u32)(HD * DM * 2) - (768u * j * DM + 160u * i) * 2u);
  u32 voffO[5];
#pragma unroll
  for (int v = 0; v < 5; ++v) { const u32 z = 64u * v + lane; voffO[v] = (z / 20u) * (DM * 2) + (z % 20u) * 16u; }
  auto wo_tile_of = [&](u32 n) { u32 t = n + 3u * i; return t >= 12u ? t - 12u : t; };
  auto wo_so = [&](u32 n) { return (wo_tile_of(n) * 64u + 16u * w) * (DM * 2); };
  auto wo_piece = [&](int n, u32 so, int v) { dma16<W_NT>(ldsb + ((n + 2) & 3) * WOS + w * 5120u + v * 1024u, voffO[v], srdO, so); };

  constexpr float THR = 8.0f;
  f32x16 o[6];
#pragma unroll
  for (int t = 0; t < 6; ++t) o[t] = f32x16{};
  wait_bar<12>();
  u32 kb[4], vb[2];
#pragma unroll
  for (int q = 0; q < 4; ++q) kb[q] = baseK0 ^ (u32)(q << 5);
  vb[0] = baseV0; vb[1] = baseV0 ^ 64u;
  auto rdK = [&](int slot, int st) { return lds_b128(kb[st & 3] + slot * SLOT + (st >> 2) * 128); };
  struct VF { s16x4 lo, hi; };
  auto rdV = [&](int slot, int n) { const int t = n % 6, hf = n / 6; const u32 a = vb[t & 1] + slot * SLOT + (t >> 1) * 128 + hf * (16 * 384); VF f; f.lo = lds_tr(a); f.hi = lds_tr(a + 8 * 384); return f; };
  const u32 dmaB = ldsb + w * 3072u;
  const rsrc_t srdV2 = mk_rsrc((const char*)p.vc[br] + (size_t)(b * NH + h) * SK * DH * 2 - 2 * 12288, SK * DH * 2 + 2 * 12288);
  u32 soK = 3 * 12288u + w * 3072u;
  f32x16 sa, sb, negm;
  float l = 0.f;
  bf16x8 kw[4];
  {
    bf16x8 kf[12];
#pragma unroll
    for (int n = 0; n < 6; ++n) kf[n] = rdK(0, n);
    PIN();
    sa = f32x16{};
#pragma unroll
    for (int n = 0; n < 12; ++n) { MF(sa, kf[n], qf[n]); if (n < 6) kf[n + 6] = rdK(0, n + 6); PIN(); }
    wait_bar_lgkm<6>();
#pragma unroll
    for (int v = 0; v < 3; ++v) dma16<KV_NT>(dmaB + v * 1024u, voffK[v], srdK, soK);
    soK += 12288u;
#pragma unroll
    for (int n = 0; n < 4; ++n) kw[n] = rdK(1, n);
    float pm = max3(sa[0], sa[1], sa[2]);
#pragma unroll
    for (int k = 3; k < 15; k += 2) pm = max3(pm, sa[k], sa[k + 1]);
    pm = __builtin_elementwise_maximum(pm, sa[15]);
    { auto rr = __builtin_amdgcn_permlane32_swap(__float_as_uint(pm), __float_as_uint(pm), false, false); pm = __builtin_elementwise_maximum(__uint_as_float(rr[0]), __uint_as_float(rr[1])); }
#pragma unroll
    for (int k = 0; k < 16; ++k) { sa[k] -= pm; negm[k] = -pm; }
    asm volatile("" : "+v"(negm));
    wait_lgkm<2>();
    PIN();
  }
  auto att_body = [&](auto MODEc, auto S3c, f32x16& sc, f32x16& sn) {
    constexpr int MODE = decltype(MODEc)::value;
    constexpr int S0 = decltype(S3c)::value, S1 = (S0 + 1) % 3, S2 = (S0 + 2) % 3;
    constexpr bool QK = MODE < 15, KNEXT = MODE < 14, DV = MODE < 14, DK = MODE == 0, WO = MODE >= 14;
    u32 pw[8];
    auto E = [&](int k) { sc[k] = __builtin_amdgcn_exp2f(sc[k]); };
    auto C = [&](int q) { pw[q] = cvtpk(sc[2 * q], sc[2 * q + 1]); };
    auto A = [&](int k) { l += sc[k]; };
    bf16x8 kf[12];
    VF vf[12];
    u32 sow = 0;
    if constexpr (WO) sow = wo_so(MODE - 14);
    auto D0 = [&](int q) {
      if constexpr (WO) { if (q < 5) set_m0(ldsb + w * 5120u, ((MODE - 14 + 2) & 3) * WOS + q * 1024); }
      else { if (q < 3) { if (DV) set_m0(dmaB, S2 * SLOT + 12288 + q * 1024); } else { if (DK) set_m0(dmaB, S1 * SLOT + (q - 3) * 1024); } }
    };
    auto D1 = [&](int q) {
      if constexpr (WO) { if (q < 5) dma_go<W_NT>(voffO[q], srdO, sow); }
      else { if (q < 3) { if (DV) dma_go<KV_NT>(voffV[q], srdV2, soK); } else { if (DK) dma_go<KV_NT>(voffK[q - 3], srdK, soK); } }
    };
    float pm = 0.f;
    auto M = [&](int q) { if constexpr (QK) { if (q == 0) pm = max3(sn[0], sn[1], sn[2]); else if (q < 7) pm = max3(pm, sn[2 * q + 1], sn[2 * q + 2]); else pm = __builtin_elementwise_maximum(pm, sn[15]); } };
#pragma unroll
    for (int n = 0; n < 4; ++n) kf[n] = kw[n];
#pragma unroll
    for (int n = 0; n < 12; ++n) {
      if constexpr (QK) {
        if (n == 0) sn = MFMA(kf[0], qf[0], negm); else sn = MFMA(kf[n], qf[n], sn);
        asm volatile("" : "+v"(sn));
        if (n < 8) kf[n + 4] = rdK(S1, n + 4);
      }
      if (n < 8) {
        if ((n & 1) == 0) { E(n); E(n + 1); }
        else { C(n >> 1); A(n - 1); A(n); if constexpr (QK) wait_lgkm<2>(); }
      }
      if (n == 8) { E(8); E(9); }
      if (n == 9) {
        if constexpr (MODE <= 12) wait_bar_lgkm<6>(); else if constexpr (MODE <= 14) wait_bar_lgkm<3>(); else wait_bar_lgkm<5>();
        vf[0] = rdV(S0, 0); vf[1] = rdV(S0, 1);
      }
      if (n == 10) { vf[2] = rdV(S0, 2); D0(0); C(4); E(10); }
      if (n == 11) { vf[3] = rdV(S0, 3); D1(0); E(11); wait_lgkm<4>(); }
      PIN();
    }
    const bf16x8 pf0 = __builtin_bit_cast(bf16x8, u32x4{pw[0], pw[1], pw[2], pw[3]});
    bool resc = false;
#pragma unroll
    for (int n = 0; n < 12; ++n) {
      if (n < 6) { MF(o[n], comb(vf[n].lo, vf[n].hi), pf0); }
      else { const bf16x8 pf1 = __builtin_bit_cast(bf16x8, u32x4{pw[4], pw[5], pw[6], pw[7]}); MF(o[n - 6], comb(vf[n].lo, vf[n].hi), pf1); }
      if (n < 8) vf[n + 4] = rdV(S0, n + 4);
      else if constexpr (KNEXT) kw[n - 8] = rdK(S2, n - 8);
      if (n == 0) { D0(1); C(5); E(12); }
      if (n == 1) { D1(1); E(13); wait_lgkm<4>(); }
      if (n == 2) { D0(2); C(6); E(14); }
      if (n == 3) { D1(2); E(15); wait_lgkm<4>(); }
      if (n == 4) { D0(3); C(7); M(0); }
      if (n == 5) { D1(3); M(1); wait_lgkm<4>(); }
      if (n == 6) { D0(4); M(2); M(3); }
      if (n == 7) { D1(4); M(4); A(8); wait_lgkm<4>(); }
      if (n == 8) { D0(5); M(5); M(6); A(9); }
      if (n == 9) { D1(5); M(7); A(10); if constexpr (KNEXT) wait_lgkm<2>(); else wait_lgkm<0>(); }
      if (n == 10) { if constexpr (QK) resc = __builtin_amdgcn_ballot_w64(pm > THR) != 0ull; A(11); A(12); A(13); }
      if (n == 11) { A(14); A(15); soK += 12288u; if constexpr (KNEXT) wait_lgkm<2>(); }
      PIN();
    }
    if constexpr (QK) {
      if (__builtin_expect(resc, 0)) {
        auto rr = __builtin_amdgcn_permlane32_swap(__float_as_uint(pm), __float_as_uint(pm), false, false);
        const float dl = __builtin_elementwise_maximum(__builtin_elementwise_maximum(__uint_as_float(rr[0]), __uint_as_float(rr[1])), 0.f);
        const float al = __builtin_amdgcn_exp2f(-dl);
        l *= al;
#pragma unroll
        for (int t = 0; t < 6; ++t)
#pragma unroll
          for (int k = 0; k < 16; ++k) o[t][k] *= al;
#pragma unroll
        for (int k = 0; k < 16; ++k) { sn[k] -= dl; negm[k] -= dl; }
      }
    }
  };
  for (u32 it = 0; it < 12; it += 6) {
    att_body(IC(0), IC(0), sa, sb); att_body(IC(0), IC(1), sb, sa);
    att_body(IC(0), IC(2), sa, sb); att_body(IC(0), IC(0), sb, sa);
    if (br == 0 && it + 4 == THROTTLE_IT && tid == 0) put_tag((gu64*)(wsb + WS_FA + idx * 64u), tag);
    att_body(IC(0), IC(1), sa, sb); att_body(IC(0), IC(2), sb, sa);
    if (br == 0 && it + 6 == THROTTLE_IT && tid == 0) put_tag((gu64*)(wsb + WS_FA + idx * 64u), tag);
  }
  att_body(IC(12), IC(0), sa, sb); att_body(IC(13), IC(1), sb, sa); att_body(IC(14), IC(2), sa, sb); att_body(IC(15), IC(0), sb, sa);
  if (USE_PRIO) __builtin_amdgcn_s_setprio(0);
  { auto rr = __builtin_amdgcn_permlane32_swap(__float_as_uint(l), __float_as_uint(l), false, false); l = __uint_as_float(rr[0]) + __uint_as_float(rr[1]); }
  const float inv = 1.0f / l;
  const rsrc_t srdOb = mk_rsrc(wsb + WS_O + (size_t)((br * NB + b) * NH) * 4 * 12288, NH * 4 * 12288);
  const u32 voffL = lane * 16u;
  u32x4 own[12];
  {
    const u32 so = (h * 4u + w) * 12288u;
#define ST_O(T) do { own[2 * T] = __builtin_bit_cast(u32x4, pack8(o[T], 0, inv)); own[2 * T + 1] = __builtin_bit_cast(u32x4, pack8(o[T], 8, inv)); \
      st16_sc1<(2 * T) * 1024 % 4096>(own[2 * T], voffL, srdOb, so + ((2 * T) * 1024 / 4096) * 4096); st16_sc1<(2 * T + 1) * 1024 % 4096>(own[2 * T + 1], voffL, srdOb, so + ((2 * T + 1) * 1024 / 4096) * 4096); } while (0)
    ST_O(0); ST_O(1); ST_O(2); ST_O(3); ST_O(4); ST_O(5);
#undef ST_O
  }

  f32x16 oa[5];
#pragma unroll
  for (int t = 0; t < 5; ++t) oa[t] = f32x16{};
  const u32 baseO = ldsb + (4u * hh + q4) * 320u + 32u * blkb + 16u * (pp & 1u) + 8u * (pp >> 1);
  auto rdO = [&](int n, int f, int hi) { return lds_tr(baseO + ((n + 2) & 3) * WOS + (f / 5) * (16 * 320) + (f % 5) * 64 + hi * (8 * 320)); };
  gu64* const FO = (gu64*)(wsb + WS_FO + (((br * NB + b) * 8u + j) * 4u + w) * 64u);
  u32x4 of[36];
  wait_bar<17>();
  s16x4 wlo[5], whi[5];
#pragma unroll
  for (int f = 0; f < 5; ++f) { wlo[f] = rdO(0, f, 0); whi[f] = rdO(0, f, 1); }
  { const u32 so = wo_so(2);
#pragma unroll
    for (int v = 0; v < 5; ++v) wo_piece(2, so, v); }
  PIN();
  auto op_tile = [&](auto Nc) {
    constexpr int N = decltype(Nc)::value;
    if constexpr (N == 3 || N == 6) {
      if constexpr (N == 3) {
        if ((WO_PF & 2) && br == 0) {
          u32 dummy; const rsrc_t srdPF = mk_rsrc((const char*)p.wo[1] + (size_t)(768u * j) * DM * 2, 768 * DM * 2);
          asm volatile("buffer_load_dword %0, %1, %2, 0 offen\n\ts_waitcnt vmcnt(0)" : "=v"(dummy) : "v"((240u * sl + tid) * 128u), "s"(srdPF) : "memory");
        }
        poll_tags(FO, 3u, lane, tag); asm volatile("" ::: "memory"); }
#pragma unroll
      for (int g = (N == 3 ? 0 : 2); g < (N == 3 ? 2 : 3); ++g) {
        const u32 so = ((4u * j + ((i + g + 1u) & 3u)) * 4u + w) * 12288u;
#pragma unroll
        for (int q = 0; q < 3; ++q) {
          ld16_sc1<0>(of[12 * g + 4 * q + 0], voffL, srdOb, so + q * 4096u); ld16_sc1<1024>(of[12 * g + 4 * q + 1], voffL, srdOb, so + q * 4096u);
          ld16_sc1<2048>(of[12 * g + 4 * q + 2], voffL, srdOb, so + q * 4096u); ld16_sc1<3072>(of[12 * g + 4 * q + 3], voffL, srdOb, so + q * 4096u);
        }
      }

    }
    if constexpr (N >= 3) {
      constexpr int B0 = 4 * (N - 3);
      constexpr int W = N == 3 ? 20 : N == 4 ? 21 : N == 5 ? 22 : 63;
      asm volatile("s_waitcnt vmcnt(%4)" : "+v"(of[B0]), "+v"(of[B0 + 1]), "+v"(of[B0 + 2]), "+v"(of[B0 + 3]) : "i"(W));
    }
    u32 so = 0;
#pragma unroll
    for (int f = 0; f < 20; ++f) {
      const u32x4 bfr = N < 3 ? own[4 * (N < 3 ? N : 0) + f / 5] : of[4 * (N >= 3 ? N - 3 : 0) + f / 5];
      MF(oa[f % 5], comb(wlo[f % 5], whi[f % 5]), __builtin_bit_cast(bf16x8, bfr));
      if (f == 15 && N < 11) {
        if constexpr (N == 0) wait_bar<17>(); else if constexpr (N == 1) wait_bar<5>(); else if constexpr (N == 2) wait_bar<5>();
        else if constexpr (N == 3 || N == 4) wait_bar<63>(); else if constexpr (N == 6 || N == 7) wait_bar<17>(); else if constexpr (N < 10) wait_bar<5>(); else wait_bar<0>();
        if constexpr (N == 1) { if (lane == 0) put_tag(FO + i, tag); }
        if constexpr (N + 3 < 12) so = wo_so(N + 3);
      }
      if (f < 15) { wlo[f % 5] = rdO(N, f + 5, 0); whi[f % 5] = rdO(N, f + 5, 1); }
      else if constexpr (N < 11) { wlo[f % 5] = rdO(N + 1, f - 15, 0); whi[f % 5] = rdO(N + 1, f - 15, 1); if constexpr (N + 3 < 12) wo_piece(N + 3, so, f - 15); }
      PIN();
    }
  };
  op_tile(IC(0)); op_tile(IC(1)); op_tile(IC(2)); op_tile(IC(3)); op_tile(IC(4)); op_tile(IC(5)); op_tile(IC(6)); op_tile(IC(7));
  op_tile(IC(8)); op_tile(IC(9)); op_tile(IC(10)); op_tile(IC(11));
  const rsrc_t srdP = mk_rsrc(wsb + WS_P + (size_t)((((br * NB + b) * 4u + i) * 4u + w) * 80u) * 1024u * PB, 80 * 1024 * PB);
#pragma unroll
  for (int u = 0; u < 10; ++u) st16_sc1<0>(__builtin_bit_cast(u32x4, pack8n(oa[u >> 1], (u & 1) * 8)), voffL, srdP, (u * 8u + j) * 1024u);
  asm volatile("s_waitcnt vmcnt(0)" ::: "memory");
  gu64* const FP = (gu64*)(wsb + WS_FP + (((br * NB + b) * 4u + i) * 4u + w) * 64u);
  if (lane == 0) put_tag(FP + j, tag);
  poll_tags(FP, 7u, lane, tag);
  asm volatile("" ::: "memory");
  for (u32 u = j; u < 10u; u += 8u) {
    float sum[8];
    u32x4 pv[8];
#pragma unroll
    for (int jj = 0; jj < 8; jj += 4) {
      ld16_sc1<0>(pv[jj], voffL, srdP, (u * 8u + jj) * 1024u); ld16_sc1<1024>(pv[jj + 1], voffL, srdP, (u * 8u + jj) * 1024u);
      ld16_sc1<2048>(pv[jj + 2], voffL, srdP, (u * 8u + jj) * 1024u); ld16_sc1<3072>(pv[jj + 3], voffL, srdP, (u * 8u + jj) * 1024u);
    }
    asm volatile("s_waitcnt vmcnt(0)" : "+v"(pv[0]), "+v"(pv[1]), "+v"(pv[2]), "+v"(pv[3]), "+v"(pv[4]), "+v"(pv[5]), "+v"(pv[6]), "+v"(pv[7]));
#pragma unroll
    for (int k = 0; k < 4; ++k) {
      float a = __uint_as_float(pv[0][k] << 16), c = __uint_as_float(pv[0][k] & 0xffff0000u);
#pragma unroll
      for (int jj = 1; jj < 8; ++jj) { a += __uint_as_float(pv[jj][k] << 16); c += __uint_as_float(pv[jj][k] & 0xffff0000u); }
      sum[2 * k] = a; sum[2 * k + 1] = c;
    }
    char* orow = (char*)p.out + ((size_t)(b * SQ + 32u * w + r) * (2 * DM) + br * DM + 160u * i + 16u * u + 8u * hh) * 2;
    u32x4 ov = {cvtpk(sum[0], sum[1]), cvtpk(sum[2], sum[3]), cvtpk(sum[4], sum[5]), cvtpk(sum[6], sum[7])};
    *(GAS u32x4*)orow = ov;
  }
  if (blk == 0 && w == 0) {
    u32 spins = 0;
    while (spins < (1u << 20)) {
      bool ok = true;
#pragma unroll
      for (int q = 0; q < 8; ++q) ok &= __hip_atomic_load((gu64*)(wsb + WS_ENTER) + q * 64 + lane, __ATOMIC_RELAXED, __HIP_MEMORY_SCOPE_AGENT) == tag;
      if (__builtin_amdgcn_ballot_w64(!ok) == 0ull) break;
      __builtin_amdgcn_s_sleep(8);
      if (++spins > (1u << 16)) break;
    }
    if (lane == 0) put_tag((gu64*)(wsb + WS_E), tag);
  }
}

extern "C" void kernel_launch(void* const* d_in, const int* in_sizes, int n_in, void* d_out, int out_size, void* d_ws, size_t ws_size, hipStream_t stream) {
  Params p;
  for (int br = 0; br < 2; ++br) {
    p.x[br] = d_in[br * 5 + 0]; p.kc[br] = d_in[br * 5 + 1]; p.vc[br] = d_in[br * 5 + 2]; p.wq[br] = d_in[br * 5 + 3]; p.wo[br] = d_in[br * 5 + 4];
  }
  p.out = d_out; p.ws = d_ws;
  hipLaunchKernelGGL(fused_kernel, dim3(512), dim3(256), 0, stream, p);
}
```

```cpp
#include <hip/hip_runtime.h>
#include <stdint.h>

#define ST_NT 0
typedef unsigned u32;
typedef unsigned long long u64;
typedef __bf16 bf16x8 __attribute__((ext_vector_type(8)));
typedef __bf16 bf16x2 __attribute__((ext_vector_type(2)));
typedef short s16x4 __attribute__((ext_vector_type(4)));
typedef short s16x8 __attribute__((ext_vector_type(8)));
typedef float f32x2 __attribute__((ext_vector_type(2)));
typedef float f32x16 __attribute__((ext_vector_type(16)));
typedef u32 u32x4 __attribute__((ext_vector_type(4)));
#define LAS __attribute__((address_space(3)))
#define GAS __attribute__((address_space(1)))
#define DI __device__ __forceinline__
typedef __amdgpu_buffer_rsrc_t rsrc_t;
typedef GAS u64 gu64;

constexpr int NB = 8, SQ = 128, DM = 640, NH = 32, SK = 512, DH = 192, HD = NH * DH;
constexpr u32 SLOT = 24576;
constexpr u32 WQS = 12288;
constexpr u32 XOFF = 49152;
constexpr u32 WOS = 20480;
constexpr u32 LDS_BYTES = 81920;
constexpr size_t WS_E = 0;
constexpr size_t WS_ENTER = 4096;
constexpr size_t WS_FA = 8192;
constexpr size_t WS_FO = 32768;
constexpr size_t WS_FP = 65536;
#define WS_BASE_MB 208
constexpr size_t WS_O = 131072 + (size_t)WS_BASE_MB * 1048576;
constexpr size_t WS_O_BYTES = (size_t)2 * NB * NH * 4 * 12288;
constexpr size_t WS_P = WS_O + WS_O_BYTES;
#define P_F32 0
constexpr u32 PB = P_F32 ? 2 : 1;
constexpr size_t WS_P_BYTES = (size_t)2 * NB * 4 * 4 * 10 * 8 * 1024 * PB;

struct Params {
  const void* x[2]; const void* kc[2]; const void* vc[2]; const void* wq[2]; const void* wo[2];
  void* out; void* ws;
};

DI u32 rfl(u32 v) { return __builtin_amdgcn_readfirstlane(v); }
DI rsrc_t mk_rsrc(const void* p, u32 bytes) { return __builtin_amdgcn_make_buffer_rsrc((void*)p, (short)0, (int)bytes, 0x00020000); }
DI u32 cvtpk(float lo, float hi) { f32x2 v = {lo, hi}; bf16x2 b = __builtin_convertvector(v, bf16x2); return __builtin_bit_cast(u32, b); }
DI bf16x8 pack8(const f32x16& a, int base, float sc) {
  u32x4 w = {cvtpk(a[base + 0] * sc, a[base + 1] * sc), cvtpk(a[base + 2] * sc, a[base + 3] * sc), cvtpk(a[base + 4] * sc, a[base + 5] * sc), cvtpk(a[base + 6] * sc, a[base + 7] * sc)};
  return __builtin_bit_cast(bf16x8, w);
}
DI bf16x8 pack8n(const f32x16& a, int base) {
  u32x4 w = {cvtpk(a[base + 0], a[base + 1]), cvtpk(a[base + 2], a[base + 3]), cvtpk(a[base + 4], a[base + 5]), cvtpk(a[base + 6], a[base + 7])};
  return __builtin_bit_cast(bf16x8, w);
}
#define MFMA(a, b, c) __builtin_amdgcn_mfma_f32_32x32x16_bf16((a), (b), (c), 0, 0, 0)
#define PIN() __builtin_amdgcn_sched_barrier(0)
#define MF(acc, a, b) do { acc = MFMA((a), (b), acc); asm volatile("" : "+v"(acc)); } while (0)

template <bool NT> DI void dma16(u32 lds, u32 voff, rsrc_t srd, u32 soff) {
  if constexpr (NT) asm volatile("s_mov_b32 m0, %0\n\ts_nop 0\n\tbuffer_load_dwordx4 %1, %2, %3 offen nt lds" :: "s"(lds), "v"(voff), "s"(srd), "s"(soff));
  else              asm volatile("s_mov_b32 m0, %0\n\ts_nop 0\n\tbuffer_load_dwordx4 %1, %2, %3 offen lds" :: "s"(lds), "v"(voff), "s"(srd), "s"(soff));
}
template <bool NT> DI void pf4(u32 lds, u32 voff, rsrc_t srd) {
  if constexpr (NT) asm volatile("s_mov_b32 m0, %0\n\ts_nop 0\n\tbuffer_load_dword %1, %2, 0 offen nt lds" :: "s"(lds), "v"(voff), "s"(srd));
  else              asm volatile("s_mov_b32 m0, %0\n\ts_nop 0\n\tbuffer_load_dword %1, %2, 0 offen lds" :: "s"(lds), "v"(voff), "s"(srd));
}
DI void set_m0(u32 base, int imm) { asm volatile("s_add_u32 m0, %0, %1" :: "s"(base), "i"(imm) : "scc"); }
template <bool NT> DI void dma_go(u32 voff, rsrc_t srd, u32 soff) {
  if constexpr (NT) asm volatile("buffer_load_dwordx4 %0, %1, %2 offen nt lds" :: "v"(voff), "s"(srd), "s"(soff));
  else              asm volatile("buffer_load_dwordx4 %0, %1, %2 offen lds" :: "v"(voff), "s"(srd), "s"(soff));
}
template <int N> DI void wait_lgkm() { __builtin_amdgcn_s_waitcnt(0xC07F | (N << 8)); }
DI float max3(float a, float b, float c) { return __builtin_elementwise_maximum(__builtin_elementwise_maximum(a, b), c); }
template <int IMM> DI void ld16_sc1(u32x4& dst, u32 voff, rsrc_t srd, u32 soff) {
  asm volatile("buffer_load_dwordx4 %0, %1, %2, %3 offen offset:%4 sc1" : "=v"(dst) : "v"(voff), "s"(srd), "s"(soff), "i"(IMM));
}
template <int IMM> DI void st16_sc1(const u32x4& src, u32 voff, rsrc_t srd, u32 soff) {
  asm volatile("buffer_store_dwordx4 %0, %1, %2, %3 offen offset:%4 sc1\n\ts_nop 1" :: "v"(src), "v"(voff), "s"(srd), "s"(soff), "i"(IMM) : "memory");
}
template <int N> DI void wait_bar() { asm volatile("s_waitcnt vmcnt(%0)\n\ts_barrier" :: "i"(N) : "memory"); }
template <int N> DI void wait_bar_lgkm() {
  asm volatile("" ::: "memory");
  __builtin_amdgcn_s_waitcnt(0x0070 | (N & 15) | ((N >> 4) << 14));
  asm volatile("s_barrier" ::: "memory");
}
DI bf16x8 lds_b128(u32 addr) { return *(const LAS bf16x8*)(size_t)addr; }
DI s16x4 lds_tr(u32 a) { return __builtin_amdgcn_ds_read_tr16_b64_v4i16((LAS s16x4*)(size_t)a); }
DI bf16x8 comb(s16x4 lo, s16x4 hi) { s16x8 v = __builtin_shufflevector(lo, hi, 0, 1, 2, 3, 4, 5, 6, 7); return __builtin_bit_cast(bf16x8, v); }
DI void put_tag(gu64* word, u64 tag) { __hip_atomic_store(word, tag, __ATOMIC_RELAXED, __HIP_MEMORY_SCOPE_AGENT); }
template <int SLEEP = 2> DI void poll_tags(gu64* words, u32 nmask, u32 lane, u64 tag) {
  u32 spins = 0;
  gu64* wp = words + (lane & nmask);
  while (true) {
    const u64 v = __hip_atomic_load(wp, __ATOMIC_RELAXED, __HIP_MEMORY_SCOPE_AGENT);
    if (__builtin_amdgcn_ballot_w64(v != tag) == 0ull) break;
    __builtin_amdgcn_s_sleep(SLEEP);
    if (++spins > (1u << 18)) break;
  }
}

#define KV_NT 1
#define W_NT 0
#define X_NT 0
#define THROTTLE_IT 12
#define USE_PRIO 1
#define B_DELAY_PER -1
#define PRO_NT 0
#define MAP_M3 1
#define A_HOLD_IT -1
#define PRIO_QA 2
#define PRIO_QB 0
#define PRIO_AA 3
#define PRIO_AB 3
#define PRIO_OA 0
#define PRIO_OB 0
#define WO_PF 0

__global__ __launch_bounds__(256, 2) void fused_kernel(Params p) {
  __shared__ __attribute__((aligned(1024))) char smem[LDS_BYTES];
  const u32 ldsb = (u32)(size_t)(LAS char*)smem;
  const u32 tid = threadIdx.x, lane = tid & 63u, w = rfl(tid >> 6);
  const u32 r = lane & 31u, hh = lane >> 5;
  const u32 blk = blockIdx.x, role = blk >> 8, idx = blk & 255u, sl = idx >> 3;
  const u32 br = (idx >> 2) & 1u, b = 4u * role + (sl >> 3), h = 8u * (idx & 3u) + (sl & 7u), j = h >> 2, i = h & 3u;
  char* const wsb = (char*)p.ws;
  if (USE_PRIO) { if (role == 0) __builtin_amdgcn_s_setprio(PRIO_QA); else __builtin_amdgcn_s_setprio(PRIO_QB); }
  u64 ev;
  { const u32 vz = 0; asm volatile("global_load_dwordx2 %0, %1, %2 sc1" : "=v"(ev) : "v"(vz), "s"(wsb)); }

  const rsrc_t srdW = mk_rsrc((const char*)p.wq[br] + h * (DH * 2), (u32)(DM * HD * 2) - h * (DH * 2));
  const rsrc_t srdX = mk_rsrc((const char*)p.x[br] + (size_t)b * SQ * DM * 2, SQ * DM * 2);
  const rsrc_t srdK = mk_rsrc((const char*)p.kc[br] + (size_t)(b * NH + h) * SK * DH * 2, SK * DH * 2);
  const rsrc_t srdV = mk_rsrc((const char*)p.vc[br] + (size_t)(b * NH + h) * SK * DH * 2, SK * DH * 2);

  u32 voffW[3], voffK[3], voffV[3], voffX[2];
#pragma unroll
  for (int v = 0; v < 3; ++v) {
    const u32 z = 64u * v + lane, rr = z / 24u, ph = z % 24u;
    const u32 cV = ph ^ (((rr >> 1) & 1u) << 2);
    const u32 cK = (ph & ~7u) | ((ph & 7u) ^ (((w & 1u) << 2) | (rr >> 1)));
    voffW[v] = rr * (HD * 2) + cV * 16u;
    voffV[v] = rr * 384u + cV * 16u;
    voffK[v] = rr * 384u + cK * 16u;
  }
#pragma unroll
  for (int v = 0; v < 2; ++v) voffX[v] = (lane >> 3) * (DM * 2) + ((lane & 7u) ^ ((u32)(v << 2) | (lane >> 4))) * 16u;
  const u32 blkb = (lane >> 4) & 1u, q4 = (lane & 15u) >> 2, pp = lane & 3u, e = (q4 >> 1) & 1u;
  const u32 bW0 = ldsb + (8u * hh + q4) * 384u + (4u * e + 2u * blkb + (pp & 1u)) * 16u + 8u * (pp >> 1), bW1 = bW0 ^ 64u;
  const u32 baseV0 = ldsb + 12288u + (4u * hh + q4) * 384u + (4u * e + 2u * blkb + (pp >> 1)) * 16u + 8u * (pp & 1u);
  const u32 baseK0 = ldsb + r * 384u + ((hh ^ ((r >> 1) & 7u)) << 4);
  const u32 baseX0 = ldsb + XOFF + w * 4096u + r * 128u + ((hh ^ ((r >> 1) & 7u)) << 4);

  const u32 c0 = ((h & 7u) + ((h >> 2) & 1u) + (5u * (b & 3u)) / 2u) % 10u;
  auto chunk_of = [&](u32 c) { u32 t = c + c0; return t >= 10u ? t - 10u : t; };
  auto wq_piece = [&](u32 slot, u32 so, int v) { dma16<W_NT>(ldsb + slot * WQS + w * 3072u + v * 1024u, voffW[v], srdW, so); };
  auto wq_piece_nt = [&](u32 slot, u32 so, int v) { dma16<true>(ldsb + slot * WQS + w * 3072u + v * 1024u, voffW[v], srdW, so); };
  auto wq_so = [&](u32 t) { return (chunk_of(t >> 1) * 64u + (t & 1u) * 32u + 8u * w) * (HD * 2); };
  auto x_piece = [&](u32 par, u32 so, int v) { dma16<X_NT>(ldsb + XOFF + par * 16384u + w * 4096u + v * 1024u, voffX[v & 1], srdX, so + v * 8u * (DM * 2)); };
  auto x_piece_nt = [&](u32 par, u32 so, int v) { dma16<true>(ldsb + XOFF + par * 16384u + w * 4096u + v * 1024u, voffX[v & 1], srdX, so + v * 8u * (DM * 2)); };
  auto x_so = [&](u32 c) { return 32u * w * (DM * 2) + chunk_of(c) * 128u; };
  auto k_piece = [&](u32 t, u32 slot_off, int v) { dma16<KV_NT>(ldsb + slot_off + w * 3072u + v * 1024u, voffK[v], srdK, t * 12288u + w * 3072u); };
  auto v_piece = [&](u32 t, u32 slot_off, int v) { dma16<KV_NT>(ldsb + slot_off + 12288u + w * 3072u + v * 1024u, voffV[v], srdV, t * 12288u + w * 3072u); };

  f32x16 acc[6];
#pragma unroll
  for (int t = 0; t < 6; ++t) acc[t] = f32x16{};
  {
    const u32 s0 = wq_so(0), s1 = wq_so(1), s2 = wq_so(2), s3 = wq_so(3), x0 = x_so(0), x1 = x_so(1);
    auto pro = [&](auto WN, auto XN) {
#pragma unroll
      for (int v = 0; v < 3; ++v) { if constexpr (decltype(WN)::value) wq_piece_nt(0, s0, v); else wq_piece(0, s0, v); }
#pragma unroll
      for (int v = 0; v < 4; ++v) { if constexpr (decltype(XN)::value) x_piece_nt(0, x0, v); else x_piece(0, x0, v); }
#pragma unroll
      for (int v = 0; v < 3; ++v) { if constexpr (decltype(WN)::value) wq_piece_nt(1, s1, v); else wq_piece(1, s1, v); }
#pragma unroll
      for (int v = 0; v < 3; ++v) { if constexpr (decltype(WN)::value) wq_piece_nt(2, s2, v); else wq_piece(2, s2, v); }
#pragma unroll
      for (int v = 0; v < 4; ++v) { if constexpr (decltype(XN)::value) x_piece_nt(1, x1, v); else x_piece(1, x1, v); }
#pragma unroll
      for (int v = 0; v < 3; ++v) { if constexpr (decltype(WN)::value) wq_piece_nt(3, s3, v); else wq_piece(3, s3, v); }
    };
    if (PRO_NT == 0) pro(std::false_type{}, std::false_type{});
    else if (role == 0) pro(std::true_type{}, std::true_type{});
    else if (PRO_NT == 3) pro(std::false_type{}, std::false_type{});
    else if (PRO_NT == 1) pro(std::false_type{}, std::true_type{});
    else pro(std::true_type{}, std::true_type{});
  }
  asm volatile("s_waitcnt vmcnt(20)" : "+v"(ev) :: "memory");
  const u64 tag = (((u64)rfl((u32)(ev >> 32)) << 32) | rfl((u32)ev)) + 1ull;
  if (B_DELAY_PER >= 0 && role == 1 && w == 0) poll_tags((gu64*)(wsb + WS_FA + idx * 64u + 8u), 0u, lane, tag);
  asm volatile("s_waitcnt vmcnt(13)\n\ts_barrier" ::: "memory");
  if (tid == 0) put_tag((gu64*)(wsb + WS_ENTER) + blk, tag);
  auto rdA = [&](int slot, int ks, int m, int hi) {
    return lds_tr(((m & 1) ? bW1 : bW0) + slot * WQS + ks * (16 * 384) + (m >> 1) * 128 + hi * (4 * 384)); };
  auto rdX = [&](int par, int xks) { return lds_b128((baseX0 ^ (u32)(xks << 5)) + par * 16384); };
  s16x4 nlo[6], nhi[6];
  bf16x8 Xn;
  auto qp_ks0 = [&](auto Sc, const s16x4 (&lo)[6], const s16x4 (&hi)[6], const bf16x8& X) {
    constexpr int S = decltype(Sc)::value, PAR = S >> 1, XK = (S & 1) * 2;
    MF(acc[0], comb(lo[0], hi[0]), X); nlo[0] = rdA(S, 1, 0, 0); nhi[0] = rdA(S, 1, 0, 1); nlo[1] = rdA(S, 1, 1, 0); PIN();
    MF(acc[1], comb(lo[1], hi[1]), X); nhi[1] = rdA(S, 1, 1, 1); nlo[2] = rdA(S, 1, 2, 0); nhi[2] = rdA(S, 1, 2, 1); PIN();
    MF(acc[2], comb(lo[2], hi[2]), X); nlo[3] = rdA(S, 1, 3, 0); nhi[3] = rdA(S, 1, 3, 1); nlo[4] = rdA(S, 1, 4, 0); PIN();
    MF(acc[3], comb(lo[3], hi[3]), X); nhi[4] = rdA(S, 1, 4, 1); nlo[5] = rdA(S, 1, 5, 0); nhi[5] = rdA(S, 1, 5, 1); PIN();
    MF(acc[4], comb(lo[4], hi[4]), X); Xn = rdX(PAR, XK + 1); PIN();
    MF(acc[5], comb(lo[5], hi[5]), X); PIN();
  };
  {
    s16x4 Alo[6], Ahi[6];
#pragma unroll
    for (int m = 0; m < 6; ++m) { Alo[m] = rdA(0, 0, m, 0); Ahi[m] = rdA(0, 0, m, 1); }
    const bf16x8 X = rdX(0, 0);
    PIN();
    qp_ks0(std::integral_constant<int, 0>{}, Alo, Ahi, X);
  }
  auto qp_tile = [&](auto TTc, auto LASTc, u32 per) {
    constexpr int TT = decltype(TTc)::value; constexpr bool LAST = decltype(LASTc)::value;
    constexpr int PAR = TT >> 1;
    if constexpr (!LAST) { if constexpr (TT & 1) wait_bar_lgkm<6>(); else wait_bar_lgkm<10>(); }
    else { if constexpr (TT == 0) wait_bar_lgkm<10>(); else if constexpr (TT < 3) wait_bar_lgkm<6>(); else wait_bar_lgkm<63>(); }
    PIN();
    if constexpr (TT == 0 && B_DELAY_PER >= 0) { if (role == 0 && per == (u32)B_DELAY_PER && tid == 0) put_tag((gu64*)(wsb + WS_FA + idx * 64u + 8u), tag); }
    const u32 tn = 4u * per + TT + 4u;
    u32 sw = 0, sx = 0;
    if constexpr (!LAST) { sw = wq_so(tn); if constexpr (TT & 1) sx = x_so(tn >> 1); }
    constexpr int NS = (TT + 1) & 3, NPAR = NS >> 1, NXK = (NS & 1) * 2;
    constexpr bool MORE = !(LAST && TT == 3);
    s16x4 Alo[6], Ahi[6]; bf16x8 X;
#pragma unroll
    for (int m = 0; m < 6; ++m) {
      MF(acc[m], comb(nlo[m], nhi[m]), Xn);
      if constexpr (MORE) { Alo[m] = rdA(NS, 0, m, 0); Ahi[m] = rdA(NS, 0, m, 1); if (m == 0) X = rdX(NPAR, NXK); }
      if constexpr (!LAST) {
        if constexpr (TT & 1) {
          if (m == 0) { x_piece(PAR, sx, 0); } if (m == 1) { x_piece(PAR, sx, 1); x_piece(PAR, sx, 2); } if (m == 2) x_piece(PAR, sx, 3);
          if (m >= 3) wq_piece(TT, sw, m - 3);
        } else { if (m & 1) wq_piece(TT, sw, m >> 1); }
      } else {
        if constexpr (TT == 0) { if (m & 1) k_piece(0, 0, m >> 1); }
        if constexpr (TT == 1) { if (m & 1) v_piece(0, 0, m >> 1); }
        if constexpr (TT == 2) { if (m < 3) k_piece(1, SLOT, m); else k_piece(2, 2 * SLOT, m - 3); }
        if constexpr (TT == 3) { if (m & 1) v_piece(1, SLOT, m >> 1); }
      }
      PIN();
    }
    if constexpr (MORE) qp_ks0(std::integral_constant<int, NS>{}, Alo, Ahi, X);
  };
  using std::integral_constant;
#define IC(n) integral_constant<int, n>{}
#define BC(n) integral_constant<bool, n>{}
  for (u32 per = 0; per < 4; ++per) { qp_tile(IC(0), BC(false), per); qp_tile(IC(1), BC(false), per); qp_tile(IC(2), BC(false), per); qp_tile(IC(3), BC(false), per); }
  qp_tile(IC(0), BC(true), 4); qp_tile(IC(1), BC(true), 4); qp_tile(IC(2), BC(true), 4); qp_tile(IC(3), BC(true), 4);
  constexpr float CSC = 0.07216878364870322f * 1.4426950408889634f;
  bf16x8 qf[12];
#pragma unroll
  for (int t = 0; t < 6; ++t) { qf[2 * t] = pack8(acc[t], 0, CSC); qf[2 * t + 1] = pack8(acc[t], 8, CSC); }
  if ((WO_PF & 4) && role == 1) {
    const rsrc_t srdPF = mk_rsrc((const char*)p.wo[br] + (size_t)(1536u * (idx & 3u)) * DM * 2, 1536 * DM * 2);
    pf4<false>(ldsb + 3 * SLOT + w * 256u, (480u * sl + tid) * 128u, srdPF);
    pf4<false>(ldsb + 3 * SLOT + 1024u + w * 256u, tid < 224u ? (480u * sl + 256u + tid) * 128u : 0x7fffff00u, srdPF);
  }
  if (role == 1 && w == 0 && THROTTLE_IT >= 0) poll_tags((gu64*)(wsb + WS_FA + idx * 64u), 0u, lane, tag);
  if (USE_PRIO) { if (role == 0) __builtin_amdgcn_s_setprio(PRIO_AA); else __builtin_amdgcn_s_setprio(PRIO_AB); }

  const rsrc_t srdO = mk_rsrc((const char*)p.wo[br] + ((size_t)(768u * j) * DM + 160u * i) * 2, (u32)(HD * DM * 2) - (768u * j * DM + 160u * i) * 2u);
  u32 voffO[5];
#pragma unroll
  for (int v = 0; v < 5; ++v) { const u32 z = 64u * v + lane; voffO[v] = (z / 20u) * (DM * 2) + (z % 20u) * 16u; }
  auto wo_tile_of = [&](u32 n) { u32 t = n + 3u * i; return t >= 12u ? t - 12u : t; };
  auto wo_so = [&](u32 n) { return (wo_tile_of(n) * 64u + 16u * w) * (DM * 2); };
  auto wo_piece = [&](int n, u32 so, int v) { dma16<W_NT>(ldsb + ((n + 2) & 3) * WOS + w * 5120u + v * 1024u, voffO[v], srdO, so); };

  constexpr float THR = 8.0f;
  f32x16 o[6];
#pragma unroll
  for (int t = 0; t < 6; ++t) o[t] = f32x16{};
  wait_bar<12>();
  u32 kb[4], vb[2];
#pragma unroll
  for (int q = 0; q < 4; ++q) kb[q] = baseK0 ^ (u32)(q << 5);
  vb[0] = baseV0; vb[1] = baseV0 ^ 64u;
  auto rdK = [&](int slot, int st) { return lds_b128(kb[st & 3] + slot * SLOT + (st >> 2) * 128); };
  struct VF { s16x4 lo, hi; };
  auto rdV = [&](int slot, int n) { const int t = n % 6, hf = n / 6; const u32 a = vb[t & 1] + slot * SLOT + (t >> 1) * 128 + hf * (16 * 384); VF f; f.lo = lds_tr(a); f.hi = lds_tr(a + 8 * 384); return f; };
  const u32 dmaB = ldsb + w * 3072u;
  const rsrc_t srdV2 = mk_rsrc((const char*)p.vc[br] + (size_t)(b * NH + h) * SK * DH * 2 - 2 * 12288, SK * DH * 2 + 2 * 12288);
  u32 soK = 3 * 12288u + w * 3072u;
  f32x16 sa, sb, negm;
  float l = 0.f;
  bf16x8 kw[4];
  {
    bf16x8 kf[12];
#pragma unroll
    for (int n = 0; n < 6; ++n) kf[n] = rdK(0, n);
    PIN();
    sa = f32x16{};
#pragma unroll
    for (int n = 0; n < 12; ++n) { MF(sa, kf[n], qf[n]); if (n < 6) kf[n + 6] = rdK(0, n + 6); PIN(); }
    wait_bar_lgkm<6>();
#pragma unroll
    for (int v = 0; v < 3; ++v) dma16<KV_NT>(dmaB + v * 1024u, voffK[v], srdK, soK);
    soK += 12288u;
#pragma unroll
    for (int n = 0; n < 4; ++n) kw[n] = rdK(1, n);
    float pm = max3(sa[0], sa[1], sa[2]);
#pragma unroll
    for (int k = 3; k < 15; k += 2) pm = max3(pm, sa[k], sa[k + 1]);
    pm = __builtin_elementwise_maximum(pm, sa[15]);
    { auto rr = __builtin_amdgcn_permlane32_swap(__float_as_uint(pm), __float_as_uint(pm), false, false); pm = __builtin_elementwise_maximum(__uint_as_float(rr[0]), __uint_as_float(rr[1])); }
#pragma unroll
    for (int k = 0; k < 16; ++k) { sa[k] -= pm; negm[k] = -pm; }
    asm volatile("" : "+v"(negm));
    wait_lgkm<2>();
    PIN();
  }
  auto att_body = [&](auto MODEc, auto S3c, f32x16& sc, f32x16& sn) {
    constexpr int MODE = decltype(MODEc)::value;
    constexpr int S0 = decltype(S3c)::value, S1 = (S0 + 1) % 3, S2 = (S0 + 2) % 3;
    constexpr bool QK = MODE < 15, KNEXT = MODE < 14, DV = MODE < 14, DK = MODE == 0, WO = MODE >= 14;
    u32 pw[8];
    auto E = [&](int k) { sc[k] = __builtin_amdgcn_exp2f(sc[k]); };
    auto C = [&](int q) { pw[q] = cvtpk(sc[2 * q], sc[2 * q + 1]); };
    auto A = [&](int k) { l += sc[k]; };
    bf16x8 kf[12];
    VF vf[12];
    u32 sow = 0;
    if constexpr (WO) sow = wo_so(MODE - 14);
    auto D0 = [&](int q) {
      if constexpr (WO) { if (q < 5) set_m0(ldsb + w * 5120u, ((MODE - 14 + 2) & 3) * WOS + q * 1024); }
      else { if (q < 3) { if (DV) set_m0(dmaB, S2 * SLOT + 12288 + q * 1024); } else { if (DK) set_m0(dmaB, S1 * SLOT + (q - 3) * 1024); } }
    };
    auto D1 = [&](int q) {
      if constexpr (WO) { if (q < 5) dma_go<W_NT>(voffO[q], srdO, sow); }
      else { if (q < 3) { if (DV) dma_go<KV_NT>(voffV[q], srdV2, soK); } else { if (DK) dma_go<KV_NT>(voffK[q - 3], srdK, soK); } }
    };
    float pm = 0.f;
    auto M = [&](int q) { if constexpr (QK) { if (q == 0) pm = max3(sn[0], sn[1], sn[2]); else if (q < 7) pm = max3(pm, sn[2 * q + 1], sn[2 * q + 2]); else pm = __builtin_elementwise_maximum(pm, sn[15]); } };
#pragma unroll
    for (int n = 0; n < 4; ++n) kf[n] = kw[n];
#pragma unroll
    for (int n = 0; n < 12; ++n) {
      if constexpr (QK) {
        if (n == 0) sn = MFMA(kf[0], qf[0], negm); else sn = MFMA(kf[n], qf[n], sn);
        asm volatile("" : "+v"(sn));
        if (n < 8) kf[n + 4] = rdK(S1, n + 4);
      }
      if (n < 8) {
        if ((n & 1) == 0) { E(n); E(n + 1); }
        else { C(n >> 1); A(n - 1); A(n); if constexpr (QK) wait_lgkm<2>(); }
      }
      if (n == 8) { E(8); E(9); }
      if (n == 9) {
        if constexpr (MODE <= 12) wait_bar_lgkm<6>(); else if constexpr (MODE <= 14) wait_bar_lgkm<3>(); else wait_bar_lgkm<5>();
        vf[0] = rdV(S0, 0); vf[1] = rdV(S0, 1);
      }
      if (n == 10) { vf[2] = rdV(S0, 2); D0(0); C(4); E(10); }
      if (n == 11) { vf[3] = rdV(S0, 3); D1(0); E(11); wait_lgkm<4>(); }
      PIN();
    }
    const bf16x8 pf0 = __builtin_bit_cast(bf16x8, u32x4{pw[0], pw[1], pw[2], pw[3]});
    bool resc = false;
#pragma unroll
    for (int n = 0; n < 12; ++n) {
      if (n < 6) { MF(o[n], comb(vf[n].lo, vf[n].hi), pf0); }
      else { const bf16x8 pf1 = __builtin_bit_cast(bf16x8, u32x4{pw[4], pw[5], pw[6], pw[7]}); MF(o[n - 6], comb(vf[n].lo, vf[n].hi), pf1); }
      if (n < 8) vf[n + 4] = rdV(S0, n + 4);
      else if constexpr (KNEXT) kw[n - 8] = rdK(S2, n - 8);
      if (n == 0) { D0(1); C(5); E(12); }
      if (n == 1) { D1(1); E(13); wait_lgkm<4>(); }
      if (n == 2) { D0(2); C(6); E(14); }
      if (n == 3) { D1(2); E(15); wait_lgkm<4>(); }
      if (n == 4) { D0(3); C(7); M(0); }
      if (n == 5) { D1(3); M(1); wait_lgkm<4>(); }
      if (n == 6) { D0(4); M(2); M(3); }
      if (n == 7) { D1(4); M(4); A(8); wait_lgkm<4>(); }
      if (n == 8) { D0(5); M(5); M(6); A(9); }
      if (n == 9) { D1(5); M(7); A(10); if constexpr (KNEXT) wait_lgkm<2>(); else wait_lgkm<0>(); }
      if (n == 10) { if constexpr (QK) resc = __builtin_amdgcn_ballot_w64(pm > THR) != 0ull; A(11); A(12); A(13); }
      if (n == 11) { A(14); A(15); soK += 12288u; if constexpr (KNEXT) wait_lgkm<2>(); }
      PIN();
    }
    if constexpr (QK) {
      if (__builtin_expect(resc, 0)) {
        auto rr = __builtin_amdgcn_permlane32_swap(__float_as_uint(pm), __float_as_uint(pm), false, false);
        const float dl = __builtin_elementwise_maximum(__builtin_elementwise_maximum(__uint_as_float(rr[0]), __uint_as_float(rr[1])), 0.f);
        const float al = __builtin_amdgcn_exp2f(-dl);
        l *= al;
#pragma unroll
        for (int t = 0; t < 6; ++t)
#pragma unroll
          for (int k = 0; k < 16; ++k) o[t][k] *= al;
#pragma unroll
        for (int k = 0; k < 16; ++k) { sn[k] -= dl; negm[k] -= dl; }
      }
    }
  };
  for (u32 it = 0; it < 12; it += 6) {
    att_body(IC(0), IC(0), sa, sb); att_body(IC(0), IC(1), sb, sa);
    att_body(IC(0), IC(2), sa, sb); att_body(IC(0), IC(0), sb, sa);
    if (A_HOLD_IT >= 0 && role == 1 && it + 4 == A_HOLD_IT && tid == 0) put_tag((gu64*)(wsb + WS_FA + idx * 64u + 16u), tag);
    if (role == 0 && it + 4 == THROTTLE_IT && tid == 0) put_tag((gu64*)(wsb + WS_FA + idx * 64u), tag);
    att_body(IC(0), IC(1), sa, sb); att_body(IC(0), IC(2), sb, sa);
    if (role == 0 && it + 6 == THROTTLE_IT && tid == 0) put_tag((gu64*)(wsb + WS_FA + idx * 64u), tag);
    if (A_HOLD_IT >= 0 && role == 1 && it + 6 == A_HOLD_IT && tid == 0) put_tag((gu64*)(wsb + WS_FA + idx * 64u + 16u), tag);
  }
  att_body(IC(12), IC(0), sa, sb); att_body(IC(13), IC(1), sb, sa);
  if (A_HOLD_IT == 14 && role == 1 && tid == 0) put_tag((gu64*)(wsb + WS_FA + idx * 64u + 16u), tag);
  att_body(IC(14), IC(2), sa, sb); att_body(IC(15), IC(0), sb, sa);
  if (A_HOLD_IT == 16 && role == 1 && tid == 0) put_tag((gu64*)(wsb + WS_FA + idx * 64u + 16u), tag);
  if (USE_PRIO) { if (role == 0) __builtin_amdgcn_s_setprio(PRIO_OA); else __builtin_amdgcn_s_setprio(PRIO_OB); }
  { auto rr = __builtin_amdgcn_permlane32_swap(__float_as_uint(l), __float_as_uint(l), false, false); l = __uint_as_float(rr[0]) + __uint_as_float(rr[1]); }
  const float inv = 1.0f / l;
  const rsrc_t srdOb = mk_rsrc(wsb + WS_O + (size_t)((br * NB + b) * NH) * 4 * 12288, NH * 4 * 12288);
  const u32 voffL = lane * 16u;
  u32x4 own[12];
  {
    const u32 so = (h * 4u + w) * 12288u;
#define ST_O(T) do { own[2 * T] = __builtin_bit_cast(u32x4, pack8(o[T], 0, inv)); own[2 * T + 1] = __builtin_bit_cast(u32x4, pack8(o[T], 8, inv)); \
      st16_sc1<(2 * T) * 1024 % 4096>(own[2 * T], voffL, srdOb, so + ((2 * T) * 1024 / 4096) * 4096); st16_sc1<(2 * T + 1) * 1024 % 4096>(own[2 * T + 1], voffL, srdOb, so + ((2 * T + 1) * 1024 / 4096) * 4096); } while (0)
    ST_O(0); ST_O(1); ST_O(2); ST_O(3); ST_O(4); ST_O(5);
#undef ST_O
  }

  f32x16 oa[5];
#pragma unroll
  for (int t = 0; t < 5; ++t) oa[t] = f32x16{};
  const u32 baseO = ldsb + (4u * hh + q4) * 320u + 32u * blkb + 16u * (pp & 1u) + 8u * (pp >> 1);
  auto rdO = [&](int n, int f, int hi) { return lds_tr(baseO + ((n + 2) & 3) * WOS + (f / 5) * (16 * 320) + (f % 5) * 64 + hi * (8 * 320)); };
  gu64* const FO = (gu64*)(wsb + WS_FO + (((br * NB + b) * 8u + j) * 4u + w) * 64u);
  u32x4 of[36];
  wait_bar<17>();
  s16x4 wlo[5], whi[5];
#pragma unroll
  for (int f = 0; f < 5; ++f) { wlo[f] = rdO(0, f, 0); whi[f] = rdO(0, f, 1); }
  { const u32 so = wo_so(2);
#pragma unroll
    for (int v = 0; v < 5; ++v) wo_piece(2, so, v); }
  PIN();
  auto op_tile = [&](auto Nc) {
    constexpr int N = decltype(Nc)::value;
    if constexpr (N == 3 || N == 6) {
      if constexpr (N == 3) {
        if (!MAP_M3 && (WO_PF & 2) && role == 0) {
          u32 dummy; const rsrc_t srdPF = mk_rsrc((const char*)p.wo[1] + (size_t)(768u * j) * DM * 2, 768 * DM * 2);
          asm volatile("buffer_load_dword %0, %1, %2, 0 offen\n\ts_waitcnt vmcnt(0)" : "=v"(dummy) : "v"((240u * sl + tid) * 128u), "s"(srdPF) : "memory");
        }
        if (A_HOLD_IT >= 0 && role == 0) {
          if (w == 0) poll_tags<16>((gu64*)(wsb + WS_FA + idx * 64u + 16u), 0u, lane, tag);
          asm volatile("s_barrier" ::: "memory");
        }
        poll_tags(FO, 3u, lane, tag); asm volatile("" ::: "memory"); }
#pragma unroll
      for (int g = (N == 3 ? 0 : 2); g < (N == 3 ? 2 : 3); ++g) {
        const u32 so = ((4u * j + ((i + g + 1u) & 3u)) * 4u + w) * 12288u;
#pragma unroll
        for (int q = 0; q < 3; ++q) {
          ld16_sc1<0>(of[12 * g + 4 * q + 0], voffL, srdOb, so + q * 4096u); ld16_sc1<1024>(of[12 * g + 4 * q + 1], voffL, srdOb, so + q * 4096u);
          ld16_sc1<2048>(of[12 * g + 4 * q + 2], voffL, srdOb, so + q * 4096u); ld16_sc1<3072>(of[12 * g + 4 * q + 3], voffL, srdOb, so + q * 4096u);
        }
      }

    }
    if constexpr (N >= 3) {
      constexpr int B0 = 4 * (N - 3);
      constexpr int W = N == 3 ? 20 : N == 4 ? 21 : N == 5 ? 22 : 63;
      asm volatile("s_waitcnt vmcnt(%4)" : "+v"(of[B0]), "+v"(of[B0 + 1]), "+v"(of[B0 + 2]), "+v"(of[B0 + 3]) : "i"(W));
    }
    u32 so = 0;
#pragma unroll
    for (int f = 0; f < 20; ++f) {
      const u32x4 bfr = N < 3 ? own[4 * (N < 3 ? N : 0) + f / 5] : of[4 * (N >= 3 ? N - 3 : 0) + f / 5];
      MF(oa[f % 5], comb(wlo[f % 5], whi[f % 5]), __builtin_bit_cast(bf16x8, bfr));
      if (f == 15 && N < 11) {
        if constexpr (N == 0) wait_bar<17>(); else if constexpr (N == 1) wait_bar<5>(); else if constexpr (N == 2) wait_bar<5>();
        else if constexpr (N == 3 || N == 4) wait_bar<63>(); else if constexpr (N == 6 || N == 7) wait_bar<17>(); else if constexpr (N < 10) wait_bar<5>(); else wait_bar<0>();
        if constexpr (N == 1) { if (lane == 0) put_tag(FO + i, tag); }
        if constexpr (N + 3 < 12) so = wo_so(N + 3);
      }
      if (f < 15) { wlo[f % 5] = rdO(N, f + 5, 0); whi[f % 5] = rdO(N, f + 5, 1); }
      else if constexpr (N < 11) { wlo[f % 5] = rdO(N + 1, f - 15, 0); whi[f % 5] = rdO(N + 1, f - 15, 1); if constexpr (N + 3 < 12) wo_piece(N + 3, so, f - 15); }
      PIN();
    }
  };
  op_tile(IC(0)); op_tile(IC(1)); op_tile(IC(2)); op_tile(IC(3)); op_tile(IC(4)); op_tile(IC(5)); op_tile(IC(6)); op_tile(IC(7));
  op_tile(IC(8)); op_tile(IC(9)); op_tile(IC(10)); op_tile(IC(11));
  const rsrc_t srdP = mk_rsrc(wsb + WS_P + (size_t)((((br * NB + b) * 4u + i) * 4u + w) * 80u) * 1024u * PB, 80 * 1024 * PB);
#pragma unroll
  for (int u = 0; u < 10; ++u) st16_sc1<0>(__builtin_bit_cast(u32x4, pack8n(oa[u >> 1], (u & 1) * 8)), voffL, srdP, (u * 8u + j) * 1024u);
  asm volatile("s_waitcnt vmcnt(0)" ::: "memory");
  gu64* const FP = (gu64*)(wsb + WS_FP + (((br * NB + b) * 4u + i) * 4u + w) * 64u);
  if (lane == 0) put_tag(FP + j, tag);
  poll_tags(FP, 7u, lane, tag);
  asm volatile("" ::: "memory");
  for (u32 u = j; u < 10u; u += 8u) {
    float sum[8];
    u32x4 pv[8];
#pragma unroll
    for (int jj = 0; jj < 8; jj += 4) {
      ld16_sc1<0>(pv[jj], voffL, srdP, (u * 8u + jj) * 1024u); ld16_sc1<1024>(pv[jj + 1], voffL, srdP, (u * 8u + jj) * 1024u);
      ld16_sc1<2048>(pv[jj + 2], voffL, srdP, (u * 8u + jj) * 1024u); ld16_sc1<3072>(pv[jj + 3], voffL, srdP, (u * 8u + jj) * 1024u);
    }
    asm volatile("s_waitcnt vmcnt(0)" : "+v"(pv[0]), "+v"(pv[1]), "+v"(pv[2]), "+v"(pv[3]), "+v"(pv[4]), "+v"(pv[5]), "+v"(pv[6]), "+v"(pv[7]));
#pragma unroll
    for (int k = 0; k < 4; ++k) {
      float a = __uint_as_float(pv[0][k] << 16), c = __uint_as_float(pv[0][k] & 0xffff0000u);
#pragma unroll
      for (int jj = 1; jj < 8; ++jj) { a += __uint_as_float(pv[jj][k] << 16); c += __uint_as_float(pv[jj][k] & 0xffff0000u); }
      sum[2 * k] = a; sum[2 * k + 1] = c;
    }
    char* orow = (char*)p.out + ((size_t)(b * SQ + 32u * w + r) * (2 * DM) + br * DM + 160u * i + 16u * u + 8u * hh) * 2;
    u32x4 ov = {cvtpk(sum[0], sum[1]), cvtpk(sum[2], sum[3]), cvtpk(sum[4], sum[5]), cvtpk(sum[6], sum[7])};
    *(GAS u32x4*)orow = ov;
  }
  if (blk == 0 && w == 0) {
    u32 spins = 0;
    while (spins < (1u << 20)) {
      bool ok = true;
#pragma unroll
      for (int q = 0; q < 8; ++q) ok &= __hip_atomic_load((gu64*)(wsb + WS_ENTER) + q * 64 + lane, __ATOMIC_RELAXED, __HIP_MEMORY_SCOPE_AGENT) == tag;
      if (__builtin_amdgcn_ballot_w64(!ok) == 0ull) break;
      __builtin_amdgcn_s_sleep(8);
      if (++spins > (1u << 16)) break;
    }
    if (lane == 0) put_tag((gu64*)(wsb + WS_E), tag);
  }
}

extern "C" void kernel_launch(void* const* d_in, const int* in_sizes, int n_in, void* d_out, int out_size, void* d_ws, size_t ws_size, hipStream_t stream) {
  Params p;
  for (int br = 0; br < 2; ++br) {
    p.x[br] = d_in[br * 5 + 0]; p.kc[br] = d_in[br * 5 + 1]; p.vc[br] = d_in[br * 5 + 2]; p.wq[br] = d_in[br * 5 + 3]; p.wo[br] = d_in[br * 5 + 4];
  }
  p.out = d_out; p.ws = d_ws;
  hipLaunchKernelGGL(fused_kernel, dim3(512), dim3(256), 0, stream, p);
}
```

```cpp
#include <hip/hip_runtime.h>
#include <stdint.h>

#define ST_NT 0
typedef unsigned u32;
typedef unsigned long long u64;
typedef __bf16 bf16x8 __attribute__((ext_vector_type(8)));
typedef __bf16 bf16x2 __attribute__((ext_vector_type(2)));
typedef short s16x4 __attribute__((ext_vector_type(4)));
typedef short s16x8 __attribute__((ext_vector_type(8)));
typedef float f32x2 __attribute__((ext_vector_type(2)));
typedef float f32x16 __attribute__((ext_vector_type(16)));
typedef u32 u32x4 __attribute__((ext_vector_type(4)));
#define LAS __attribute__((address_space(3)))
#define GAS __attribute__((address_space(1)))
#define DI __device__ __forceinline__
typedef __amdgpu_buffer_rsrc_t rsrc_t;
typedef GAS u64 gu64;

constexpr int NB = 8, SQ = 128, DM = 640, NH = 32, SK = 512, DH = 192, HD = NH * DH;
constexpr u32 SLOT = 24576;
constexpr u32 WQS = 12288;
constexpr u32 XOFF = 49152;
constexpr u32 WOS = 20480;
constexpr u32 LDS_BYTES = 81920;
constexpr size_t WS_E = 0;
constexpr size_t WS_ENTER = 4096;
constexpr size_t WS_FA = 8192;
constexpr size_t WS_FO = 32768;
constexpr size_t WS_FP = 65536;
#define WS_BASE_MB 208
constexpr size_t WS_O = 131072 + (size_t)WS_BASE_MB * 1048576;
constexpr size_t WS_O_BYTES = (size_t)2 * NB * NH * 4 * 12288;
constexpr size_t WS_P = WS_O + WS_O_BYTES;
constexpr size_t WS_P_BYTES = (size_t)2 * NB * 4 * 4 * 10 * 4 * 1024;

struct Params {
  const void* x[2]; const void* kc[2]; const void* vc[2]; const void* wq[2]; const void* wo[2];
  void* out; void* ws;
};

DI u32 rfl(u32 v) { return __builtin_amdgcn_readfirstlane(v); }
DI rsrc_t mk_rsrc(const void* p, u32 bytes) { return __builtin_amdgcn_make_buffer_rsrc((void*)p, (short)0, (int)bytes, 0x00020000); }
DI u32 cvtpk(float lo, float hi) { f32x2 v = {lo, hi}; bf16x2 b = __builtin_convertvector(v, bf16x2); return __builtin_bit_cast(u32, b); }
DI bf16x8 pack8(const f32x16& a, int base, float sc) {
  u32x4 w = {cvtpk(a[base + 0] * sc, a[base + 1] * sc), cvtpk(a[base + 2] * sc, a[base + 3] * sc), cvtpk(a[base + 4] * sc, a[base + 5] * sc), cvtpk(a[base + 6] * sc, a[base + 7] * sc)};
  return __builtin_bit_cast(bf16x8, w);
}
DI bf16x8 pack8n(const f32x16& a, int base) {
  u32x4 w = {cvtpk(a[base + 0], a[base + 1]), cvtpk(a[base + 2], a[base + 3]), cvtpk(a[base + 4], a[base + 5]), cvtpk(a[base + 6], a[base + 7])};
  return __builtin_bit_cast(bf16x8, w);
}
#define MFMA(a, b, c) __builtin_amdgcn_mfma_f32_32x32x16_bf16((a), (b), (c), 0, 0, 0)
#define PIN() __builtin_amdgcn_sched_barrier(0)
#define MF(acc, a, b) do { acc = MFMA((a), (b), acc); asm volatile("" : "+v"(acc)); } while (0)

template <bool NT> DI void dma16(u32 lds, u32 voff, rsrc_t srd, u32 soff) {
  if constexpr (NT) asm volatile("s_mov_b32 m0, %0\n\ts_nop 0\n\tbuffer_load_dwordx4 %1, %2, %3 offen nt lds" :: "s"(lds), "v"(voff), "s"(srd), "s"(soff));
  else              asm volatile("s_mov_b32 m0, %0\n\ts_nop 0\n\tbuffer_load_dwordx4 %1, %2, %3 offen lds" :: "s"(lds), "v"(voff), "s"(srd), "s"(soff));
}
template <bool NT> DI void pf4(u32 lds, u32 voff, rsrc_t srd) {
  if constexpr (NT) asm volatile("s_mov_b32 m0, %0\n\ts_nop 0\n\tbuffer_load_dword %1, %2, 0 offen nt lds" :: "s"(lds), "v"(voff), "s"(srd));
  else              asm volatile("s_mov_b32 m0, %0\n\ts_nop 0\n\tbuffer_load_dword %1, %2, 0 offen lds" :: "s"(lds), "v"(voff), "s"(srd));
}
DI void set_m0(u32 base, int imm) { asm volatile("s_add_u32 m0, %0, %1" :: "s"(base), "i"(imm) : "scc"); }
template <bool NT> DI void dma_go(u32 voff, rsrc_t srd, u32 soff) {
  if constexpr (NT) asm volatile("buffer_load_dwordx4 %0, %1, %2 offen nt lds" :: "v"(voff), "s"(srd), "s"(soff));
  else              asm volatile("buffer_load_dwordx4 %0, %1, %2 offen lds" :: "v"(voff), "s"(srd), "s"(soff));
}
template <int N> DI void wait_lgkm() { __builtin_amdgcn_s_waitcnt(0xC07F | (N << 8)); }
DI float max3(float a, float b, float c) { return __builtin_elementwise_maximum(__builtin_elementwise_maximum(a, b), c); }
template <int IMM> DI void ld16_sc1(u32x4& dst, u32 voff, rsrc_t srd, u32 soff) {
  asm volatile("buffer_load_dwordx4 %0, %1, %2, %3 offen offset:%4 sc1" : "=v"(dst) : "v"(voff), "s"(srd), "s"(soff), "i"(IMM));
}
template <int IMM> DI void ld16_sc1_rw(u32x4& dst, u32 voff, rsrc_t srd, u32 soff) {
  asm volatile("buffer_load_dwordx4 %0, %1, %2, %3 offen offset:%4 sc1" : "+v"(dst) : "v"(voff), "s"(srd), "s"(soff), "i"(IMM));
}
template <int IMM> DI void st16_sc1(const u32x4& src, u32 voff, rsrc_t srd, u32 soff) {
  asm volatile("buffer_store_dwordx4 %0, %1, %2, %3 offen offset:%4 sc1\n\ts_nop 1" :: "v"(src), "v"(voff), "s"(srd), "s"(soff), "i"(IMM) : "memory");
}
template <int IMM> DI void st16_o(const u32x4& src, u32 voff, rsrc_t srd, u32 soff) {
  st16_sc1<IMM>(src, voff, srd, soff);
}
DI void ld4_touch_s(u32& dst, u32 voff, rsrc_t srd, u32 soff) { asm volatile("buffer_load_dword %0, %1, %2, %3 offen" : "+v"(dst) : "v"(voff), "s"(srd), "s"(soff)); }
#define PF_POL ""
DI void ld4_touch(u32& dst, u32 voff, rsrc_t srd) { asm volatile("buffer_load_dword %0, %1, %2, 0 offen" PF_POL : "+v"(dst) : "v"(voff), "s"(srd)); }
template <int N> DI void wait_vm(u32x4& a) { asm volatile("s_waitcnt vmcnt(%1)" : "+v"(a) : "i"(N)); }
template <int N> DI void wait_vm(u64& a) { asm volatile("s_waitcnt vmcnt(%1)" : "+v"(a) : "i"(N)); }
DI void ld8_sc1(u64& dst, u32 voff, gu64* base) { asm volatile("global_load_dwordx2 %0, %1, %2 sc1" : "=v"(dst) : "v"(voff), "s"(base)); }
template <int K> DI void flags_again(u64& fl, u32 voff, gu64* base, u64 tag) {
  u32 cnt;
  asm volatile("s_waitcnt vmcnt(%5)\n\tv_cmp_ne_u64_e32 vcc, %4, %0\n\ts_cbranch_vccz 2f\n\ts_mov_b32 %1, 0x40000\n"
               "1:\n\tglobal_load_dwordx2 %0, %2, %3 sc1\n\ts_waitcnt vmcnt(0)\n\tv_cmp_ne_u64_e32 vcc, %4, %0\n\ts_cbranch_vccz 2f\n\t"
               "s_sleep 2\n\ts_sub_u32 %1, %1, 1\n\ts_cmp_lg_u32 %1, 0\n\ts_cbranch_scc1 1b\n2:"
               : "+v"(fl), "=&s"(cnt) : "v"(voff), "s"(base), "s"(tag), "i"(K) : "vcc", "scc", "memory");
}
DI void head_again(u32x4* f, u32 early, u32 fvoff, gu64* base, u64 tag, u32 voff, rsrc_t srd, u32 s0, u32 s1, u32 s2) {
  u32 cnt; u64 fl;
  asm volatile("s_cmp_lg_u32 %[early], 0\n\ts_cbranch_scc1 3f\n\ts_mov_b32 %[cnt], 0x40000\n"
               "1:\n\tglobal_load_dwordx2 %[fl], %[fvoff], %[base] sc1\n\ts_waitcnt vmcnt(0)\n\tv_cmp_ne_u64_e32 vcc, %[tag], %[fl]\n\ts_cbranch_vccz 2f\n\t"
               "s_sleep 2\n\ts_sub_u32 %[cnt], %[cnt], 1\n\ts_cmp_lg_u32 %[cnt], 0\n\ts_cbranch_scc1 1b\n"
               "2:\n\t"
               "buffer_load_dwordx4 %0, %[voff], %[srd], %[s0] offen sc1\n\tbuffer_load_dwordx4 %1, %[voff], %[srd], %[s0] offen offset:1024 sc1\n\t"
               "buffer_load_dwordx4 %2, %[voff], %[srd], %[s0] offen offset:2048 sc1\n\tbuffer_load_dwordx4 %3, %[voff], %[srd], %[s0] offen offset:3072 sc1\n\t"
               "buffer_load_dwordx4 %4, %[voff], %[srd], %[s1] offen sc1\n\tbuffer_load_dwordx4 %5, %[voff], %[srd], %[s1] offen offset:1024 sc1\n\t"
               "buffer_load_dwordx4 %6, %[voff], %[srd], %[s1] offen offset:2048 sc1\n\tbuffer_load_dwordx4 %7, %[voff], %[srd], %[s1] offen offset:3072 sc1\n\t"
               "buffer_load_dwordx4 %8, %[voff], %[srd], %[s2] offen sc1\n\tbuffer_load_dwordx4 %9, %[voff], %[srd], %[s2] offen offset:1024 sc1\n\t"
               "buffer_load_dwordx4 %10, %[voff], %[srd], %[s2] offen offset:2048 sc1\n\tbuffer_load_dwordx4 %11, %[voff], %[srd], %[s2] offen offset:3072 sc1\n\t"
               "s_waitcnt vmcnt(0)\n3:"
               : "+v"(f[0]), "+v"(f[1]), "+v"(f[2]), "+v"(f[3]), "+v"(f[4]), "+v"(f[5]), "+v"(f[6]), "+v"(f[7]), "+v"(f[8]), "+v"(f[9]), "+v"(f[10]), "+v"(f[11]),
                 [cnt] "=&s"(cnt), [fl] "=&v"(fl)
               : [early] "s"(early), [fvoff] "v"(fvoff), [base] "s"(base), [tag] "s"(tag), [voff] "v"(voff), [srd] "s"(srd), [s0] "s"(s0), [s1] "s"(s1), [s2] "s"(s2)
               : "vcc", "scc", "memory");
}
template <int N> DI void wait_bar() { asm volatile("s_waitcnt vmcnt(%0)\n\ts_barrier" :: "i"(N) : "memory"); }
template <int N> DI void wait_bar_lgkm() {
  asm volatile("" ::: "memory");
  __builtin_amdgcn_s_waitcnt(0x0070 | (N & 15) | ((N >> 4) << 14));
  asm volatile("s_barrier" ::: "memory");
}
DI bf16x8 lds_b128(u32 addr) { return *(const LAS bf16x8*)(size_t)addr; }
DI s16x4 lds_tr(u32 a) { return __builtin_amdgcn_ds_read_tr16_b64_v4i16((LAS s16x4*)(size_t)a); }
DI bf16x8 comb(s16x4 lo, s16x4 hi) { s16x8 v = __builtin_shufflevector(lo, hi, 0, 1, 2, 3, 4, 5, 6, 7); return __builtin_bit_cast(bf16x8, v); }
DI void put_tag(gu64* word, u64 tag) { __hip_atomic_store(word, tag, __ATOMIC_RELAXED, __HIP_MEMORY_SCOPE_AGENT); }
template <int SLEEP = 2> DI void poll_tags(gu64* words, u32 nmask, u32 lane, u64 tag) {
  u32 spins = 0;
  gu64* wp = words + (lane & nmask);
  while (true) {
    const u64 v = __hip_atomic_load(wp, __ATOMIC_RELAXED, __HIP_MEMORY_SCOPE_AGENT);
    if (__builtin_amdgcn_ballot_w64(v != tag) == 0ull) break;
    __builtin_amdgcn_s_sleep(SLEEP);
    if (++spins > (1u << 18)) break;
  }
}

#define KV_NT 1
#define W_NT 0
#define WO_NT 0
#define X_NT 0
#define THROTTLE_IT -1
#define USE_PRIO 1
#define PRIO_QA 2
#define PRIO_QB 3
#define PRIO_AA 1
#define PRIO_AB 2
#define PRIO_OA 3
#define PRIO_OB 0
#define PF_WQB -1
#define PF_WOA 0
#define PF_WOB 0
#define PF_OWN 0
#define PF_END 2
#define PF_START 0
#define PF_A 0
#define PF_B 2
#define PF_LINES 4
#define B_SLEEP 0
#define OP_BAR 15
#define OP_L1 3
#define OP_L2 4
#define OP_FLT 9
#define OP_CNT_ST 12
#define OP_EARLY 1

struct OpPlan {
  int kb[24];
  int ko[24][4];
  int ld[24][2];
  int kf, first_s;
};
constexpr int op_pieces(int f) { return f < OP_BAR ? 0 : (5 / (20 - OP_BAR)) + ((f - OP_BAR) < 5 % (20 - OP_BAR) ? 1 : 0); }
constexpr int op_ldpos(int nld, int f) {
  return nld == 1 ? ((f & 3) == 1 && f < 16 ? f >> 2 : -1) : nld == 2 ? ((f & 1) == 1 && f < 16 ? f >> 1 : -1) : -1; }
constexpr OpPlan make_plan() {
  OpPlan P{};
  int c = 0, posW[28] = {}, posO[24][4] = {}, posF = 0;
  posW[1] = 4; c = 5;
  c += 5; posW[2] = c - 1; c += 5; posW[3] = c - 1;
  for (int t = 3; t < 6; ++t) for (int q = 0; q < 4; ++q) posO[t][q] = c++;
  c += OP_CNT_ST;
  P.first_s = 15 - OP_L2;
  for (int n = 0; n < 24; ++n) {
    int k = 0; P.ld[n][0] = P.ld[n][1] = -1;
    for (int t = 6; t < 24; ++t) if (t - (t < 15 ? OP_L1 : OP_L2) == n) P.ld[n][k++] = t;
    if (n == P.first_s) P.kf = c - 1 - posF;
    for (int f = 0; f < 20; ++f) {
      if (f % 5 == 0) { const int y = n >= 3 ? c - 1 - posO[n][f / 5] : 63; P.ko[n][f / 5] = y > 63 ? 63 : y; }
      if (f == OP_BAR && n < 23) { const int y = c - 1 - posW[n + 1]; P.kb[n] = y > 63 ? 63 : y; if (n == OP_FLT) posF = c++; }
      const int lp = op_ldpos(k, f);
      if (lp >= 0) posO[P.ld[n][lp >> 2]][lp & 3] = c++;
      if (n + 4 < 24) { c += op_pieces(f); posW[n + 4] = c - 1; }
    }
  }
  return P;
}
constexpr OpPlan PLAN = make_plan();
static_assert(PLAN.kb[3] <= PLAN.kb[0], "");

__global__ __launch_bounds__(256, 2) void fused_kernel(Params p) {
  __shared__ __attribute__((aligned(1024))) char smem[LDS_BYTES];
  const u32 ldsb = (u32)(size_t)(LAS char*)smem;
  const u32 tid = threadIdx.x, lane = tid & 63u, w = rfl(tid >> 6);
  const u32 r = lane & 31u, hh = lane >> 5;
  const u32 blk = blockIdx.x, role = blk >> 8, idx = blk & 255u, sl = idx >> 3;
  const u32 br = (idx >> 2) & 1u, j8 = idx & 3u, b = sl >> 2, i = sl & 3u, j = 2u * j8 + role, h = 4u * j + i;
  char* const wsb = (char*)p.ws;
  if (USE_PRIO) { if (role == 0) __builtin_amdgcn_s_setprio(PRIO_QA); else __builtin_amdgcn_s_setprio(PRIO_QB); }
  u64 ev;
  { const u32 vz = 0; asm volatile("global_load_dwordx2 %0, %1, %2 sc1" : "=v"(ev) : "v"(vz), "s"(wsb)); }

  const rsrc_t srdW = mk_rsrc((const char*)p.wq[br] + h * (DH * 2), (u32)(DM * HD * 2) - h * (DH * 2));
  const rsrc_t srdX = mk_rsrc((const char*)p.x[br] + (size_t)b * SQ * DM * 2, SQ * DM * 2);
  const rsrc_t srdK = mk_rsrc((const char*)p.kc[br] + (size_t)(b * NH + h) * SK * DH * 2, SK * DH * 2);
  const rsrc_t srdV = mk_rsrc((const char*)p.vc[br] + (size_t)(b * NH + h) * SK * DH * 2, SK * DH * 2);

  u32 voffW[3], voffK[3], voffV[3], voffX[2];
#pragma unroll
  for (int v = 0; v < 3; ++v) {
    const u32 z = 64u * v + lane, rr = z / 24u, ph = z % 24u;
    const u32 cV = ph ^ (((rr >> 1) & 1u) << 2);
    const u32 cK = (ph & ~7u) | ((ph & 7u) ^ (((w & 1u) << 2) | (rr >> 1)));
    voffW[v] = rr * (HD * 2) + cV * 16u;
    voffV[v] = rr * 384u + cV * 16u;
    voffK[v] = rr * 384u + cK * 16u;
  }
#pragma unroll
  for (int v = 0; v < 2; ++v) voffX[v] = (lane >> 3) * (DM * 2) + ((lane & 7u) ^ ((u32)(v << 2) | (lane >> 4))) * 16u;
  const u32 blkb = (lane >> 4) & 1u, q4 = (lane & 15u) >> 2, pp = lane & 3u, e = (q4 >> 1) & 1u;
  const u32 bW0 = ldsb + (8u * hh + q4) * 384u + (4u * e + 2u * blkb + (pp & 1u)) * 16u + 8u * (pp >> 1), bW1 = bW0 ^ 64u;
  const u32 baseV0 = ldsb + 12288u + (4u * hh + q4) * 384u + (4u * e + 2u * blkb + (pp >> 1)) * 16u + 8u * (pp & 1u);
  const u32 baseK0 = ldsb + r * 384u + ((hh ^ ((r >> 1) & 7u)) << 4);
  const u32 baseX0 = ldsb + XOFF + w * 4096u + r * 128u + ((hh ^ ((r >> 1) & 7u)) << 4);

  const u32 c0 = (b + (b >> 2) + 2u * i + (i >> 1)) % 10u;
  auto chunk_of = [&](u32 c) { u32 t = c + c0; return t >= 10u ? t - 10u : t; };
  auto wq_piece = [&](u32 slot, u32 so, int v) { dma16<W_NT>(ldsb + slot * WQS + w * 3072u + v * 1024u, voffW[v], srdW, so); };
  auto wq_so = [&](u32 t) { return (chunk_of(t >> 1) * 64u + (t & 1u) * 32u + 8u * w) * (HD * 2); };
  auto x_piece = [&](u32 par, u32 so, int v) { dma16<X_NT>(ldsb + XOFF + par * 16384u + w * 4096u + v * 1024u, voffX[v & 1], srdX, so + v * 8u * (DM * 2)); };
  auto x_so = [&](u32 c) { return 32u * w * (DM * 2) + chunk_of(c) * 128u; };
  auto k_piece = [&](u32 t, u32 slot_off, int v) { dma16<KV_NT>(ldsb + slot_off + w * 3072u + v * 1024u, voffK[v], srdK, t * 12288u + w * 3072u); };
  auto v_piece = [&](u32 t, u32 slot_off, int v) { dma16<KV_NT>(ldsb + slot_off + 12288u + w * 3072u + v * 1024u, voffV[v], srdV, t * 12288u + w * 3072u); };

  f32x16 acc[6];
#pragma unroll
  for (int t = 0; t < 6; ++t) acc[t] = f32x16{};
  if (role == 1) {
    const u64 t0 = __builtin_amdgcn_s_memrealtime();
    u32 d0 = 0, d1 = 0;
    if (PF_WQB >= 0) {
      while (__builtin_amdgcn_s_memrealtime() - t0 < (u64)(PF_WQB)) __builtin_amdgcn_s_sleep(4);
      const u32 z = 240u * sl + (tid < 240u ? tid : 0u), row = z / 12u, ln = z % 12u;
      const rsrc_t srdPF = mk_rsrc((const char*)p.wq[br] + (size_t)(4u * j) * DH * 2, (u32)(DM * HD * 2) - 4u * j * (DH * 2));
      asm volatile("buffer_load_dword %0, %1, %2, 0 offen" : "=v"(d0) : "v"(row * (HD * 2) + ln * 128u), "s"(srdPF));
      if (PF_WOA == 2) {
        const rsrc_t srdP2 = mk_rsrc((const char*)p.wo[br] + (size_t)(1536u * j8) * DM * 2, 768 * DM * 2);
        asm volatile("buffer_load_dword %0, %1, %2, 0 offen" : "=v"(d1) : "v"((240u * sl + (tid < 240u ? tid : 0u)) * 128u), "s"(srdP2));
      }
    }
    while (__builtin_amdgcn_s_memrealtime() - t0 < (u64)(B_SLEEP)) __builtin_amdgcn_s_sleep(4);
    if (PF_WOA == 1) {
      const rsrc_t srdPF = mk_rsrc((const char*)p.wo[br] + (size_t)(1536u * j8) * DM * 2, 768 * DM * 2);
      asm volatile("buffer_load_dword %0, %1, %2, 0 offen" : "=v"(d1) : "v"((240u * sl + (tid < 240u ? tid : 0u)) * 128u), "s"(srdPF));
    }
    asm volatile("s_waitcnt vmcnt(0)" : "+v"(d0), "+v"(d1) :: "memory");
  }
  {
    const u32 s0 = wq_so(0), s1 = wq_so(1), s2 = wq_so(2), s3 = wq_so(3), x0 = x_so(0), x1 = x_so(1);
#pragma unroll
    for (int v = 0; v < 3; ++v) wq_piece(0, s0, v);
#pragma unroll
    for (int v = 0; v < 4; ++v) x_piece(0, x0, v);
#pragma unroll
    for (int v = 0; v < 3; ++v) wq_piece(1, s1, v);
#pragma unroll
    for (int v = 0; v < 3; ++v) wq_piece(2, s2, v);
#pragma unroll
    for (int v = 0; v < 4; ++v) x_piece(1, x1, v);
#pragma unroll
    for (int v = 0; v < 3; ++v) wq_piece(3, s3, v);
  }
  u32 ps0 = 0, ps1 = 0;
  auto touch_wo = [&]() {
    const rsrc_t srdP = mk_rsrc((const char*)p.wo[br] + (size_t)(1536u * j8 + 768u * role) * DM * 2, 768 * DM * 2);
    ld4_touch(ps0, (240u * sl + (tid < 240u ? tid : 0u)) * 128u, srdP);
  };
  auto touch_wo2 = [&](u32 who) {
    const rsrc_t srdP = mk_rsrc((const char*)p.wo[br] + (size_t)(1536u * j8) * DM * 2, role == who ? 1536 * DM * 2 : 0);
    const u32 vo = (240u * sl + (tid < 240u ? tid : 0u)) * 128u;
    ld4_touch(ps0, vo, srdP); ld4_touch(ps0, vo + 7680u * 128u, srdP);
  };
  if (PF_START == 2) touch_wo();
  if (PF_START == 14) touch_wo2(0);
  if (PF_START == 15) touch_wo2(1);
  const rsrc_t srdT = mk_rsrc((const char*)p.wo[br] + (size_t)(1536u * j8 + 768u * role) * DM * 2, 768 * DM * 2);
  const u32 voffT = lane < PF_LINES ? (240u * sl + 60u * w + lane) * 128u : 0x7fff0000u;
  if (PF_START == 1) {
    const u32 z = 240u * sl + (tid < 240u ? tid : 0u), row = z / 12u, ln = z % 12u;
    const rsrc_t srdP1 = mk_rsrc((const char*)p.wq[br] + (size_t)(4u * j) * DH * 2, (u32)(DM * HD * 2) - 4u * j * (DH * 2));
    asm volatile("buffer_load_dword %0, %1, %2, 0 offen" : "=v"(ps0) : "v"(row * (HD * 2) + ln * 128u), "s"(srdP1));
    const rsrc_t srdP2 = mk_rsrc((const char*)p.x[br], NB * SQ * DM * 2);
    asm volatile("buffer_load_dword %0, %1, %2, 0 offen" : "=v"(ps1) : "v"(((role * 32u + sl) * 160u + (tid < 160u ? tid : 0u)) * 128u), "s"(srdP2));
  }
  asm volatile("s_waitcnt vmcnt(%1)" : "+v"(ev) : "i"(PF_START == 1 ? 22 : 20) : "memory");
  const u64 tag = (((u64)rfl((u32)(ev >> 32)) << 32) | rfl((u32)ev)) + 1ull;
  asm volatile("s_waitcnt vmcnt(%0)\n\ts_barrier" :: "i"(PF_START == 1 ? 15 : 13) : "memory");
  if (tid == 0) put_tag((gu64*)(wsb + WS_ENTER) + blk, tag);
  auto rdA = [&](int slot, int ks, int m, int hi) {
    return lds_tr(((m & 1) ? bW1 : bW0) + slot * WQS + ks * (16 * 384) + (m >> 1) * 128 + hi * (4 * 384)); };
  auto rdX = [&](int par, int xks) { return lds_b128((baseX0 ^ (u32)(xks << 5)) + par * 16384); };
  s16x4 nlo[6], nhi[6];
  bf16x8 Xn;
  auto qp_ks0 = [&](auto Sc, const s16x4 (&lo)[6], const s16x4 (&hi)[6], const bf16x8& X) {
    constexpr int S = decltype(Sc)::value, PAR = S >> 1, XK = (S & 1) * 2;
    MF(acc[0], comb(lo[0], hi[0]), X); nlo[0] = rdA(S, 1, 0, 0); nhi[0] = rdA(S, 1, 0, 1); nlo[1] = rdA(S, 1, 1, 0); PIN();
    MF(acc[1], comb(lo[1], hi[1]), X); nhi[1] = rdA(S, 1, 1, 1); nlo[2] = rdA(S, 1, 2, 0); nhi[2] = rdA(S, 1, 2, 1); PIN();
    MF(acc[2], comb(lo[2], hi[2]), X); nlo[3] = rdA(S, 1, 3, 0); nhi[3] = rdA(S, 1, 3, 1); nlo[4] = rdA(S, 1, 4, 0); PIN();
    MF(acc[3], comb(lo[3], hi[3]), X); nhi[4] = rdA(S, 1, 4, 1); nlo[5] = rdA(S, 1, 5, 0); nhi[5] = rdA(S, 1, 5, 1); PIN();
    MF(acc[4], comb(lo[4], hi[4]), X); Xn = rdX(PAR, XK + 1); PIN();
    MF(acc[5], comb(lo[5], hi[5]), X); PIN();
  };
  {
    s16x4 Alo[6], Ahi[6];
#pragma unroll
    for (int m = 0; m < 6; ++m) { Alo[m] = rdA(0, 0, m, 0); Ahi[m] = rdA(0, 0, m, 1); }
    const bf16x8 X = rdX(0, 0);
    PIN();
    qp_ks0(std::integral_constant<int, 0>{}, Alo, Ahi, X);
  }
  auto qp_tile = [&](auto TTc, auto LASTc, u32 per) {
    constexpr int TT = decltype(TTc)::value; constexpr bool LAST = decltype(LASTc)::value;
    constexpr int PAR = TT >> 1;
    if constexpr (!LAST) { if constexpr (TT & 1) wait_bar_lgkm<6>(); else wait_bar_lgkm<10>(); }
    else { if constexpr (TT == 0) wait_bar_lgkm<10>(); else if constexpr (TT < 3) wait_bar_lgkm<6>(); else wait_bar_lgkm<63>(); }
    PIN();
    const u32 tn = 4u * per + TT + 4u;
    u32 sw = 0, sx = 0;
    if constexpr (!LAST) { sw = wq_so(tn); if constexpr (TT & 1) sx = x_so(tn >> 1); }
    constexpr int NS = (TT + 1) & 3, NPAR = NS >> 1, NXK = (NS & 1) * 2;
    constexpr bool MORE = !(LAST && TT == 3);
    s16x4 Alo[6], Ahi[6]; bf16x8 X;
#pragma unroll
    for (int m = 0; m < 6; ++m) {
      MF(acc[m], comb(nlo[m], nhi[m]), Xn);
      if constexpr (MORE) { Alo[m] = rdA(NS, 0, m, 0); Ahi[m] = rdA(NS, 0, m, 1); if (m == 0) X = rdX(NPAR, NXK); }
      if constexpr (!LAST) {
        if constexpr (TT & 1) {
          if (m == 0) { x_piece(PAR, sx, 0); } if (m == 1) { x_piece(PAR, sx, 1); x_piece(PAR, sx, 2); } if (m == 2) x_piece(PAR, sx, 3);
          if (m >= 3) wq_piece(TT, sw, m - 3);
        } else { if (m & 1) wq_piece(TT, sw, m >> 1); }
      } else {
        if constexpr (TT == 0) { if (m & 1) k_piece(0, 0, m >> 1); }
        if constexpr (TT == 1) { if (m & 1) v_piece(0, 0, m >> 1); }
        if constexpr (TT == 2) { if (m < 3) k_piece(1, SLOT, m); else k_piece(2, 2 * SLOT, m - 3); }
        if constexpr (TT == 3) { if (m & 1) v_piece(1, SLOT, m >> 1); }
      }
      PIN();
    }
    if constexpr (!LAST && PF_START == 9) { if (PF_LINES == 4 || 4u * per + TT >= 16 - 60 / PF_LINES) ld4_touch_s(ps0, voffT, srdT, (4u * per + TT - (PF_LINES == 4 ? 0 : 16 - 60 / PF_LINES)) * (PF_LINES * 128)); PIN(); }
    if constexpr (MORE) qp_ks0(std::integral_constant<int, NS>{}, Alo, Ahi, X);
  };
  using std::integral_constant;
#define IC(n) integral_constant<int, n>{}
#define BC(n) integral_constant<bool, n>{}
  for (u32 per = 0; per < 4; ++per) { if (PF_START >= 3 && PF_START < 8 && per == PF_START - 3) touch_wo(); if (PF_START == 8 && per == (role == 0 ? PF_A : PF_B)) touch_wo(); if (PF_START >= 10 && PF_START < 14 && per == PF_START - 10) touch_wo2(1); qp_tile(IC(0), BC(false), per); qp_tile(IC(1), BC(false), per); qp_tile(IC(2), BC(false), per); qp_tile(IC(3), BC(false), per); }
  qp_tile(IC(0), BC(true), 4); qp_tile(IC(1), BC(true), 4); qp_tile(IC(2), BC(true), 4); qp_tile(IC(3), BC(true), 4);
  if (PF_START) asm volatile("" : "+v"(ps0), "+v"(ps1));
  constexpr float CSC = 0.07216878364870322f * 1.4426950408889634f;
  bf16x8 qf[12];
#pragma unroll
  for (int t = 0; t < 6; ++t) { qf[2 * t] = pack8(acc[t], 0, CSC); qf[2 * t + 1] = pack8(acc[t], 8, CSC); }
  if (PF_WOA >= 3 && role == 1) {
    const rsrc_t srdPF = mk_rsrc((const char*)p.wo[br] + (size_t)(1536u * j8) * DM * 2, 1536 * DM * 2);
    pf4<false>(ldsb + 3 * SLOT + w * 256u, (240u * sl + (tid < 240u ? tid : 0u)) * 128u, srdPF);
    if (PF_WOA == 4) pf4<false>(ldsb + 3 * SLOT + 1024u + w * 256u, (7680u + 240u * sl + (tid < 240u ? tid : 0u)) * 128u, srdPF);
  }
  if (role == 1 && w == 0 && THROTTLE_IT >= 0) poll_tags((gu64*)(wsb + WS_FA + idx * 64u), 0u, lane, tag);
  if (USE_PRIO) { if (role == 0) __builtin_amdgcn_s_setprio(PRIO_AA); else __builtin_amdgcn_s_setprio(PRIO_AB); }

  const rsrc_t srdO = mk_rsrc((const char*)p.wo[br] + ((size_t)(1536u * j8) * DM + 160u * i) * 2, role == 0 ? (u32)(HD * DM * 2) - (1536u * j8 * DM + 160u * i) * 2u : 0u);
  u32 voffO[5];
#pragma unroll
  for (int v = 0; v < 5; ++v) { const u32 z = 64u * v + lane; voffO[v] = (z / 20u) * (DM * 2) + (z % 20u) * 16u; }
  auto wo_so = [&](u32 half, u32 n) { u32 t = n + 3u * i; t = t >= 12u ? t - 12u : t; return ((half * 12u + t) * 64u + 16u * w) * (DM * 2); };
  auto wo_piece = [&](int n, u32 so, int v) { dma16<WO_NT>(ldsb + ((n + 2) & 3) * WOS + w * 5120u + v * 1024u, voffO[v], srdO, so); };

  constexpr float THR = 8.0f;
  f32x16 o[6];
#pragma unroll
  for (int t = 0; t < 6; ++t) o[t] = f32x16{};
  wait_bar<12>();
  u32 kb[4], vb[2];
#pragma unroll
  for (int q = 0; q < 4; ++q) kb[q] = baseK0 ^ (u32)(q << 5);
  vb[0] = baseV0; vb[1] = baseV0 ^ 64u;
  auto rdK = [&](int slot, int st) { return lds_b128(kb[st & 3] + slot * SLOT + (st >> 2) * 128); };
  struct VF { s16x4 lo, hi; };
  auto rdV = [&](int slot, int n) { const int t = n % 6, hf = n / 6; const u32 a = vb[t & 1] + slot * SLOT + (t >> 1) * 128 + hf * (16 * 384); VF f; f.lo = lds_tr(a); f.hi = lds_tr(a + 8 * 384); return f; };
  const u32 dmaB = ldsb + w * 3072u;
  const rsrc_t srdV2 = mk_rsrc((const char*)p.vc[br] + (size_t)(b * NH + h) * SK * DH * 2 - 2 * 12288, SK * DH * 2 + 2 * 12288);
  u32 soK = 3 * 12288u + w * 3072u;
  f32x16 sa, sb, negm;
  float l = 0.f;
  bf16x8 kw[4];
  {
    bf16x8 kf[12];
#pragma unroll
    for (int n = 0; n < 6; ++n) kf[n] = rdK(0, n);
    PIN();
    sa = f32x16{};
#pragma unroll
    for (int n = 0; n < 12; ++n) { MF(sa, kf[n], qf[n]); if (n < 6) kf[n + 6] = rdK(0, n + 6); PIN(); }
    wait_bar_lgkm<6>();
#pragma unroll
    for (int v = 0; v < 3; ++v) dma16<KV_NT>(dmaB + v * 1024u, voffK[v], srdK, soK);
    soK += 12288u;
#pragma unroll
    for (int n = 0; n < 4; ++n) kw[n] = rdK(1, n);
    float pm = max3(sa[0], sa[1], sa[2]);
#pragma unroll
    for (int k = 3; k < 15; k += 2) pm = max3(pm, sa[k], sa[k + 1]);
    pm = __builtin_elementwise_maximum(pm, sa[15]);
    { auto rr = __builtin_amdgcn_permlane32_swap(__float_as_uint(pm), __float_as_uint(pm), false, false); pm = __builtin_elementwise_maximum(__uint_as_float(rr[0]), __uint_as_float(rr[1])); }
#pragma unroll
    for (int k = 0; k < 16; ++k) { sa[k] -= pm; negm[k] = -pm; }
    asm volatile("" : "+v"(negm));
    wait_lgkm<2>();
    PIN();
  }
  gu64* const FO = (gu64*)(wsb + WS_FO + (((br * NB + b) * 8u + j) * 4u + w) * 64u);
  u64 flg;
  auto att_body = [&](auto MODEc, auto S3c, f32x16& sc, f32x16& sn) {
    constexpr int MODE = decltype(MODEc)::value;
    constexpr int S0 = decltype(S3c)::value, S1 = (S0 + 1) % 3, S2 = (S0 + 2) % 3;
    constexpr bool QK = MODE < 15, KNEXT = MODE < 14, DV = MODE < 14, DK = MODE == 0, WO = MODE >= 14;
    u32 pw[8];
    auto E = [&](int k) { sc[k] = __builtin_amdgcn_exp2f(sc[k]); };
    auto C = [&](int q) { pw[q] = cvtpk(sc[2 * q], sc[2 * q + 1]); };
    auto A = [&](int k) { l += sc[k]; };
    bf16x8 kf[12];
    VF vf[12];
    u32 sow = 0;
    if constexpr (WO) sow = wo_so(role, MODE - 14);
    auto D0 = [&](int q) {
      if constexpr (WO) { if (q < 5) set_m0(ldsb + w * 5120u, ((MODE - 14 + 2) & 3) * WOS + q * 1024); }
      else { if (q < 3) { if (DV) set_m0(dmaB, S2 * SLOT + 12288 + q * 1024); } else { if (DK) set_m0(dmaB, S1 * SLOT + (q - 3) * 1024); } }
    };
    auto D1 = [&](int q) {
      if constexpr (WO) { if (q < 5) dma_go<WO_NT>(voffO[q], srdO, sow); }
      else { if (q < 3) { if (DV) dma_go<KV_NT>(voffV[q], srdV2, soK); } else { if (DK) dma_go<KV_NT>(voffK[q - 3], srdK, soK); } }
    };
    float pm = 0.f;
    auto M = [&](int q) { if constexpr (QK) { if (q == 0) pm = max3(sn[0], sn[1], sn[2]); else if (q < 7) pm = max3(pm, sn[2 * q + 1], sn[2 * q + 2]); else pm = __builtin_elementwise_maximum(pm, sn[15]); } };
#pragma unroll
    for (int n = 0; n < 4; ++n) kf[n] = kw[n];
#pragma unroll
    for (int n = 0; n < 12; ++n) {
      if constexpr (QK) {
        if (n == 0) sn = MFMA(kf[0], qf[0], negm); else sn = MFMA(kf[n], qf[n], sn);
        asm volatile("" : "+v"(sn));
        if (n < 8) kf[n + 4] = rdK(S1, n + 4);
      }
      if (n < 8) {
        if ((n & 1) == 0) { E(n); E(n + 1); }
        else { C(n >> 1); A(n - 1); A(n); if constexpr (QK) wait_lgkm<2>(); }
      }
      if (n == 8) { E(8); E(9); }
      if (n == 9) {
        if constexpr (MODE <= 12) wait_bar_lgkm<6>(); else if constexpr (MODE <= 14) wait_bar_lgkm<3>(); else wait_bar_lgkm<5>();
        if constexpr (MODE == 15) ld8_sc1(flg, (lane & 3u) * 8u, FO + 32);
        if constexpr (MODE == 15 && (PF_END == 1 || PF_END == 2))
          pf4<PF_END == 1>(ldsb + SLOT + w * 256u, (240u * sl + (tid < 240u ? tid : 0u)) * 128u, mk_rsrc((const char*)p.wo[br] + (size_t)(1536u * j8 + 768u * role) * DM * 2, 768 * DM * 2));
        if constexpr (MODE == 15 && PF_END == 4) {
          const rsrc_t srdPF = mk_rsrc((const char*)p.wo[br] + (size_t)(1536u * j8) * DM * 2, role == 1 ? 1536 * DM * 2 : 0);
          pf4<false>(ldsb + SLOT + w * 256u, (240u * sl + (tid < 240u ? tid : 0u)) * 128u, srdPF);
          pf4<false>(ldsb + SLOT + 1024u + w * 256u, (7680u + 240u * sl + (tid < 240u ? tid : 0u)) * 128u, srdPF);
        }
        if constexpr (MODE == 15 && PF_END == 3) {
          const rsrc_t srdPF = mk_rsrc((const char*)p.wo[br] + (size_t)(1536u * j8) * DM * 2, role == 0 ? 1536 * DM * 2 : 0);
          pf4<false>(ldsb + SLOT + w * 256u, (7680u + 240u * sl + (tid < 240u ? tid : 0u)) * 128u, srdPF);
          pf4<false>(ldsb + SLOT + 1024u + w * 256u, (240u * sl + (tid < 240u ? tid : 0u)) * 128u, srdPF);
        }
        vf[0] = rdV(S0, 0); vf[1] = rdV(S0, 1);
      }
      if (n == 10) { vf[2] = rdV(S0, 2); D0(0); C(4); E(10); }
      if (n == 11) { vf[3] = rdV(S0, 3); D1(0); E(11); wait_lgkm<4>(); }
      PIN();
    }
    const bf16x8 pf0 = __builtin_bit_cast(bf16x8, u32x4{pw[0], pw[1], pw[2], pw[3]});
    bool resc = false;
#pragma unroll
    for (int n = 0; n < 12; ++n) {
      if (n < 6) { MF(o[n], comb(vf[n].lo, vf[n].hi), pf0); }
      else { const bf16x8 pf1 = __builtin_bit_cast(bf16x8, u32x4{pw[4], pw[5], pw[6], pw[7]}); MF(o[n - 6], comb(vf[n].lo, vf[n].hi), pf1); }
      if (n < 8) vf[n + 4] = rdV(S0, n + 4);
      else if constexpr (KNEXT) kw[n - 8] = rdK(S2, n - 8);
      if (n == 0) { D0(1); C(5); E(12); }
      if (n == 1) { D1(1); E(13); wait_lgkm<4>(); }
      if (n == 2) { D0(2); C(6); E(14); }
      if (n == 3) { D1(2); E(15); wait_lgkm<4>(); }
      if (n == 4) { D0(3); C(7); M(0); }
      if (n == 5) { D1(3); M(1); wait_lgkm<4>(); }
      if (n == 6) { D0(4); M(2); M(3); }
      if (n == 7) { D1(4); M(4); A(8); wait_lgkm<4>(); }
      if (n == 8) { D0(5); M(5); M(6); A(9); }
      if (n == 9) { D1(5); M(7); A(10); if constexpr (KNEXT) wait_lgkm<2>(); else wait_lgkm<0>(); }
      if (n == 10) { if constexpr (QK) resc = __builtin_amdgcn_ballot_w64(pm > THR) != 0ull; A(11); A(12); A(13); }
      if (n == 11) { A(14); A(15); soK += 12288u; if constexpr (KNEXT) wait_lgkm<2>(); }
      PIN();
    }
    if constexpr (QK) {
      if (__builtin_expect(resc, 0)) {
        auto rr = __builtin_amdgcn_permlane32_swap(__float_as_uint(pm), __float_as_uint(pm), false, false);
        const float dl = __builtin_elementwise_maximum(__builtin_elementwise_maximum(__uint_as_float(rr[0]), __uint_as_float(rr[1])), 0.f);
        const float al = __builtin_amdgcn_exp2f(-dl);
        l *= al;
#pragma unroll
        for (int t = 0; t < 6; ++t)
#pragma unroll
          for (int k = 0; k < 16; ++k) o[t][k] *= al;
#pragma unroll
        for (int k = 0; k < 16; ++k) { sn[k] -= dl; negm[k] -= dl; }
      }
    }
  };
  for (u32 it = 0; it < 12; it += 6) {
    att_body(IC(0), IC(0), sa, sb); att_body(IC(0), IC(1), sb, sa);
    att_body(IC(0), IC(2), sa, sb); att_body(IC(0), IC(0), sb, sa);
    if (role == 0 && it + 4 == THROTTLE_IT && tid == 0) put_tag((gu64*)(wsb + WS_FA + idx * 64u), tag);
    att_body(IC(0), IC(1), sa, sb); att_body(IC(0), IC(2), sb, sa);
    if (role == 0 && it + 6 == THROTTLE_IT && tid == 0) put_tag((gu64*)(wsb + WS_FA + idx * 64u), tag);
  }
  att_body(IC(12), IC(0), sa, sb); att_body(IC(13), IC(1), sb, sa);
  att_body(IC(14), IC(2), sa, sb); att_body(IC(15), IC(0), sb, sa);
  if (USE_PRIO) { if (role == 0) __builtin_amdgcn_s_setprio(PRIO_OA); else __builtin_amdgcn_s_setprio(PRIO_OB); }
  const rsrc_t srdOb = mk_rsrc(wsb + WS_O + (size_t)((br * NB + b) * NH) * 4 * 12288, NH * 4 * 12288);
  const u32 voffL = lane * 16u;
  auto wo_son = [&](int n) { return n < 3 ? wo_so(0u, n) : n < 15 ? wo_so(1u, n - 3) : wo_so(0u, n - 12); };
  u32x4 of[24];
  auto o_so = [&](int n) {
    const u32 head = n < 15 ? 4u * j + 4u + ((i + (u32)((n - 3) / 3)) & 3u) : 4u * j + ((i + 1u + (u32)((n - 15) / 3)) & 3u);
    return (head * 4u + w) * 12288u + (u32)(n % 3) * 4096u; };
  auto ld_frag = [&](auto Nc, auto Qc, u32 vo, u32 so) {
    constexpr int N = decltype(Nc)::value, Q = decltype(Qc)::value;
    ld16_sc1<Q * 1024>(of[4 * ((N - 3) % 6) + Q], vo, srdOb, so); };
  auto ld_tile = [&](auto Nc, u32 vo) { const u32 so = o_so(decltype(Nc)::value);
    ld_frag(Nc, IC(0), vo, so); ld_frag(Nc, IC(1), vo, so); ld_frag(Nc, IC(2), vo, so); ld_frag(Nc, IC(3), vo, so); };
  const u32 baseO = ldsb + (4u * hh + q4) * 320u + 32u * blkb + 16u * (pp & 1u) + 8u * (pp >> 1);
  auto rdO = [&](int n, int f, int hi) { return lds_tr(baseO + ((n + 2) & 3) * WOS + (f / 5) * (16 * 320) + (f % 5) * 64 + hi * (8 * 320)); };
  s16x4 wlo[5], whi[5];
  u32 early = 0;
  asm volatile("s_waitcnt vmcnt(%1)\n\ts_barrier" : "+v"(flg) : "i"(PF_END == 1 || PF_END == 2 ? 6 : PF_END >= 3 ? 7 : 5) : "memory");
  if (role == 0) {
    const u32 so = wo_son(2), so3 = wo_son(3);
#pragma unroll
    for (int v = 0; v < 5; ++v) wo_piece(2, so, v);
#pragma unroll
    for (int v = 0; v < 5; ++v) wo_piece(3, so3, v);
    early = rfl(OP_EARLY && __builtin_amdgcn_ballot_w64(flg != tag) == 0ull ? 1u : 0u);
    const u32 vo = early ? voffL : 0x7fff0000u;
    ld_tile(IC(3), vo); ld_tile(IC(4), vo); ld_tile(IC(5), vo);
  }
  PIN();
  { auto rr = __builtin_amdgcn_permlane32_swap(__float_as_uint(l), __float_as_uint(l), false, false); l = __uint_as_float(rr[0]) + __uint_as_float(rr[1]); }
  const float inv = 1.0f / l;
  u32x4 own[12];
  {
    const u32 so = (h * 4u + w) * 12288u;
#define ST_O(T) do { own[2 * T] = __builtin_bit_cast(u32x4, pack8(o[T], 0, inv)); own[2 * T + 1] = __builtin_bit_cast(u32x4, pack8(o[T], 8, inv)); \
      st16_o<(2 * T) * 1024 % 4096>(own[2 * T], voffL, srdOb, so + ((2 * T) * 1024 / 4096) * 4096); st16_o<(2 * T + 1) * 1024 % 4096>(own[2 * T + 1], voffL, srdOb, so + ((2 * T + 1) * 1024 / 4096) * 4096); } while (0)
    ST_O(0); ST_O(1); ST_O(2); ST_O(3); ST_O(4); ST_O(5);
#undef ST_O
  }
  if (role == 1) {
    asm volatile("s_waitcnt vmcnt(0)" ::: "memory");
    if (lane == 0) put_tag(FO + i, tag);
    return;
  }

  f32x16 oa[5];
#pragma unroll
  for (int t = 0; t < 5; ++t) oa[t] = f32x16{};
#pragma unroll
  for (int f = 0; f < 5; ++f) { wlo[f] = rdO(0, f, 0); whi[f] = rdO(0, f, 1); }
  u64 fls;
  PIN();
  auto lane_now = [&]() { u32 t = voffL; asm volatile("" : "+v"(t)); return t >> 4; };
  auto op_tile = [&](auto Nc) {
    constexpr int N = decltype(Nc)::value;
    constexpr int B0 = N < 3 ? 0 : 4 * ((N - 3) % 6);
    constexpr int L0 = PLAN.ld[N][0], L1 = PLAN.ld[N][1], NL = (L0 >= 0) + (L1 >= 0);
    if constexpr (N == 3) {
      head_again(of, early, (lane_now() & 3u) * 8u, FO + 32, tag, voffL, srdOb, o_so(3), o_so(4), o_so(5));
    }
    if constexpr (N == PLAN.first_s) {
      flags_again<PLAN.kf>(fls, (lane_now() & 3u) * 8u, FO, tag);
    }
    u32 so = 0, s0 = 0, s1 = 0;
    if constexpr (L0 >= 0) s0 = o_so(L0);
    if constexpr (L1 >= 0) s1 = o_so(L1);
    auto step = [&](auto Fc) {
      constexpr int f = decltype(Fc)::value;
      if constexpr (N >= 3 && f % 5 == 0 && PLAN.ko[N][f / 5] < 63) wait_vm<PLAN.ko[N][f / 5]>(of[B0 + f / 5]);
      const u32x4 bfr = N < 3 ? own[4 * (N < 3 ? N : 0) + f / 5] : of[B0 + f / 5];
      MF(oa[f % 5], comb(wlo[f % 5], whi[f % 5]), __builtin_bit_cast(bf16x8, bfr));
      if constexpr (f == OP_BAR && N < 23) {
        wait_bar_lgkm<PLAN.kb[N]>();
        if constexpr (N == 3) { if (voffL == 0) put_tag(FO + i, tag); }
        if constexpr (N == OP_FLT) ld8_sc1(fls, (lane_now() & 3u) * 8u, FO);
        if constexpr (N == 2) { }
        if constexpr (N + 4 < 24) so = wo_son(N + 4);
      }
      if constexpr (f < 15) { wlo[f % 5] = rdO(N, f + 5, 0); whi[f % 5] = rdO(N, f + 5, 1); }
      else if constexpr (N < 23 && f >= OP_BAR) {
#pragma unroll
        for (int q = (f == OP_BAR ? 0 : f - 15); q <= f - 15; ++q) { wlo[q] = rdO(N + 1, q, 0); whi[q] = rdO(N + 1, q, 1); }
      }
      constexpr int lp = op_ldpos(NL, f);
      if constexpr (lp >= 0) {
        if constexpr (lp < 4) ld_frag(IC(L0 < 3 ? 3 : L0), IC(lp & 3), voffL, s0); else ld_frag(IC(L1 < 3 ? 3 : L1), IC(lp & 3), voffL, s1); }
      if constexpr (N + 4 < 24 && f >= OP_BAR) {
        constexpr int done = [] { int d = 0; for (int x = 0; x < f; ++x) d += op_pieces(x); return d; }();
#pragma unroll
        for (int v = 0; v < op_pieces(f); ++v) {
          wo_piece(N + 4, so, done + v);
        }
      }
      PIN();
    };
    step(IC(0)); step(IC(1)); step(IC(2)); step(IC(3)); step(IC(4)); step(IC(5)); step(IC(6)); step(IC(7)); step(IC(8)); step(IC(9));
    step(IC(10)); step(IC(11)); step(IC(12)); step(IC(13)); step(IC(14)); step(IC(15)); step(IC(16)); step(IC(17)); step(IC(18)); step(IC(19));
  };
  op_tile(IC(0)); op_tile(IC(1)); op_tile(IC(2)); op_tile(IC(3)); op_tile(IC(4)); op_tile(IC(5)); op_tile(IC(6)); op_tile(IC(7));
  op_tile(IC(8)); op_tile(IC(9)); op_tile(IC(10)); op_tile(IC(11));
  op_tile(IC(12)); op_tile(IC(13)); op_tile(IC(14)); op_tile(IC(15)); op_tile(IC(16)); op_tile(IC(17)); op_tile(IC(18)); op_tile(IC(19));
  op_tile(IC(20)); op_tile(IC(21)); op_tile(IC(22)); op_tile(IC(23));
  const rsrc_t srdP = mk_rsrc(wsb + WS_P + (size_t)((((br * NB + b) * 4u + i) * 4u + w) * 40u) * 1024u, 40 * 1024);
#pragma unroll
  for (int u = 0; u < 10; ++u) st16_sc1<0>(__builtin_bit_cast(u32x4, pack8n(oa[u >> 1], (u & 1) * 8)), voffL, srdP, (u * 4u + j8) * 1024u);
  asm volatile("s_waitcnt vmcnt(0)" ::: "memory");
  gu64* const FP = (gu64*)(wsb + WS_FP + (((br * NB + b) * 4u + i) * 4u + w) * 64u);
  if (voffL == 0) put_tag(FP + j8, tag);
  const u32 ln = lane_now();
  poll_tags(FP, 3u, ln, tag);
  asm volatile("" ::: "memory");
  {
    u32x4 pv[12];
    const u32 so = j8 * 8192u, s2 = (8u + (j8 & 1u)) * 4096u;
    ld16_sc1<0>(pv[0], voffL, srdP, so); ld16_sc1<1024>(pv[1], voffL, srdP, so); ld16_sc1<2048>(pv[2], voffL, srdP, so); ld16_sc1<3072>(pv[3], voffL, srdP, so);
    ld16_sc1<0>(pv[4], voffL, srdP, so + 4096u); ld16_sc1<1024>(pv[5], voffL, srdP, so + 4096u); ld16_sc1<2048>(pv[6], voffL, srdP, so + 4096u); ld16_sc1<3072>(pv[7], voffL, srdP, so + 4096u);
    const u32 vo2 = j8 < 2u ? voffL : 0x7fff0000u;
    ld16_sc1<0>(pv[8], vo2, srdP, s2); ld16_sc1<1024>(pv[9], vo2, srdP, s2); ld16_sc1<2048>(pv[10], vo2, srdP, s2); ld16_sc1<3072>(pv[11], vo2, srdP, s2);
    asm volatile("s_waitcnt vmcnt(0)" : "+v"(pv[0]), "+v"(pv[1]), "+v"(pv[2]), "+v"(pv[3]), "+v"(pv[4]), "+v"(pv[5]), "+v"(pv[6]), "+v"(pv[7]),
                 "+v"(pv[8]), "+v"(pv[9]), "+v"(pv[10]), "+v"(pv[11]));
    auto red = [&](int q) {
      u32x4 ov;
#pragma unroll
      for (int k = 0; k < 4; ++k) {
        float a = __uint_as_float(pv[q][k] << 16), c = __uint_as_float(pv[q][k] & 0xffff0000u);
#pragma unroll
        for (int jj = 1; jj < 4; ++jj) { a += __uint_as_float(pv[q + jj][k] << 16); c += __uint_as_float(pv[q + jj][k] & 0xffff0000u); }
        ov[k] = cvtpk(a, c);
      }
      return ov;
    };
    char* const ob = (char*)p.out + ((size_t)(b * SQ + 32u * w) * (2 * DM) + br * DM + 160u * i) * 2;
    const u32x4 e0 = red(0), e1 = red(4);
    u32x4 f0, f1;
#pragma unroll
    for (int k = 0; k < 4; ++k) { auto rr = __builtin_amdgcn_permlane16_swap(e0[k], e1[k], false, false); f0[k] = rr[0]; f1[k] = rr[1]; }
    char* const orow = ob + (size_t)(ln & 15u) * (4 * DM) + (32u * j8 + 16u * ((ln >> 4) & 1u) + 8u * (ln >> 5)) * 2;
    *(GAS u32x4*)orow = f0;
    *(GAS u32x4*)(orow + 16 * 4 * DM) = f1;
    if (j8 < 2u) *(GAS u32x4*)(ob + (size_t)(ln & 31u) * (4 * DM) + (16u * (8u + j8) + 8u * (ln >> 5)) * 2) = red(8);
  }
  if (blk == 0 && w == 0) {
    u32 spins = 0;
    while (spins < (1u << 20)) {
      bool ok = true;
#pragma unroll
      for (int q = 0; q < 8; ++q) ok &= __hip_atomic_load((gu64*)(wsb + WS_ENTER) + q * 64 + ln, __ATOMIC_RELAXED, __HIP_MEMORY_SCOPE_AGENT) == tag;
      if (__builtin_amdgcn_ballot_w64(!ok) == 0ull) break;
      __builtin_amdgcn_s_sleep(8);
      if (++spins > (1u << 16)) break;
    }
    if (ln == 0) put_tag((gu64*)(wsb + WS_E), tag);
  }
}

extern "C" void kernel_launch(void* const* d_in, const int* in_sizes, int n_in, void* d_out, int out_size, void* d_ws, size_t ws_size, hipStream_t stream) {
  Params p;
  for (int br = 0; br < 2; ++br) {
    p.x[br] = d_in[br * 5 + 0]; p.kc[br] = d_in[br * 5 + 1]; p.vc[br] = d_in[br * 5 + 2]; p.wq[br] = d_in[br * 5 + 3]; p.wo[br] = d_in[br * 5 + 4];
  }
  p.out = d_out; p.ws = d_ws;
  hipLaunchKernelGGL(fused_kernel, dim3(512), dim3(256), 0, stream, p);
}
```

```cpp
#include <hip/hip_runtime.h>
#include <stdint.h>

#define ST_NT 0
typedef unsigned u32;
typedef unsigned long long u64;
typedef __bf16 bf16x8 __attribute__((ext_vector_type(8)));
typedef __bf16 bf16x2 __attribute__((ext_vector_type(2)));
typedef short s16x4 __attribute__((ext_vector_type(4)));
typedef short s16x8 __attribute__((ext_vector_type(8)));
typedef float f32x2 __attribute__((ext_vector_type(2)));
typedef float f32x16 __attribute__((ext_vector_type(16)));
typedef u32 u32x4 __attribute__((ext_vector_type(4)));
#define LAS __attribute__((address_space(3)))
#define GAS __attribute__((address_space(1)))
#define DI __device__ __forceinline__
typedef __amdgpu_buffer_rsrc_t rsrc_t;
typedef GAS u64 gu64;

constexpr int NB = 8, SQ = 128, DM = 640, NH = 32, SK = 512, DH = 192, HD = NH * DH;
constexpr u32 SLOT = 24576;
constexpr u32 WQS = 12288;
constexpr u32 XOFF = 49152;
constexpr u32 WOS = 20480;
constexpr u32 LDS_BYTES = 81920;
constexpr size_t WS_E = 0;
constexpr size_t WS_ENTER = 4096;
constexpr size_t WS_FA = 8192;
constexpr size_t WS_FO = 32768;
constexpr size_t WS_FP = 65536;
#define WS_BASE_MB 208
constexpr size_t WS_O = 131072 + (size_t)WS_BASE_MB * 1048576;
constexpr size_t WS_O_BYTES = (size_t)2 * NB * NH * 4 * 12288;
constexpr size_t WS_P = WS_O + WS_O_BYTES;
constexpr size_t WS_P_BYTES = (size_t)2 * NB * 4 * 4 * 10 * 4 * 1024;

struct Params {
  const void* x[2]; const void* kc[2]; const void* vc[2]; const void* wq[2]; const void* wo[2];
  void* out; void* ws;
};

DI u32 rfl(u32 v) { return __builtin_amdgcn_readfirstlane(v); }
DI rsrc_t mk_rsrc(const void* p, u32 bytes) { return __builtin_amdgcn_make_buffer_rsrc((void*)p, (short)0, (int)bytes, 0x00020000); }
DI u32 cvtpk(float lo, float hi) { f32x2 v = {lo, hi}; bf16x2 b = __builtin_convertvector(v, bf16x2); return __builtin_bit_cast(u32, b); }
DI bf16x8 pack8(const f32x16& a, int base, float sc) {
  u32x4 w = {cvtpk(a[base + 0] * sc, a[base + 1] * sc), cvtpk(a[base + 2] * sc, a[base + 3] * sc), cvtpk(a[base + 4] * sc, a[base + 5] * sc), cvtpk(a[base + 6] * sc, a[base + 7] * sc)};
  return __builtin_bit_cast(bf16x8, w);
}
DI bf16x8 pack8n(const f32x16& a, int base) {
  u32x4 w = {cvtpk(a[base + 0], a[base + 1]), cvtpk(a[base + 2], a[base + 3]), cvtpk(a[base + 4], a[base + 5]), cvtpk(a[base + 6], a[base + 7])};
  return __builtin_bit_cast(bf16x8, w);
}
#define MFMA(a, b, c) __builtin_amdgcn_mfma_f32_32x32x16_bf16((a), (b), (c), 0, 0, 0)
#define PIN() __builtin_amdgcn_sched_barrier(0)
#define MF(acc, a, b) do { acc = MFMA((a), (b), acc); asm volatile("" : "+v"(acc)); } while (0)

template <bool NT> DI void dma16(u32 lds, u32 voff, rsrc_t srd, u32 soff) {
  if constexpr (NT) asm volatile("s_mov_b32 m0, %0\n\ts_nop 0\n\tbuffer_load_dwordx4 %1, %2, %3 offen nt lds" :: "s"(lds), "v"(voff), "s"(srd), "s"(soff));
  else              asm volatile("s_mov_b32 m0, %0\n\ts_nop 0\n\tbuffer_load_dwordx4 %1, %2, %3 offen lds" :: "s"(lds), "v"(voff), "s"(srd), "s"(soff));
}
template <bool NT> DI void pf4(u32 lds, u32 voff, rsrc_t srd) {
  if constexpr (NT) asm volatile("s_mov_b32 m0, %0\n\ts_nop 0\n\tbuffer_load_dword %1, %2, 0 offen nt lds" :: "s"(lds), "v"(voff), "s"(srd));
  else              asm volatile("s_mov_b32 m0, %0\n\ts_nop 0\n\tbuffer_load_dword %1, %2, 0 offen lds" :: "s"(lds), "v"(voff), "s"(srd));
}
DI void set_m0(u32 base, int imm) { asm volatile("s_add_u32 m0, %0, %1" :: "s"(base), "i"(imm) : "scc"); }
template <bool NT> DI void dma_go(u32 voff, rsrc_t srd, u32 soff) {
  if constexpr (NT) asm volatile("buffer_load_dwordx4 %0, %1, %2 offen nt lds" :: "v"(voff), "s"(srd), "s"(soff));
  else              asm volatile("buffer_load_dwordx4 %0, %1, %2 offen lds" :: "v"(voff), "s"(srd), "s"(soff));
}
template <int N> DI void wait_lgkm() { __builtin_amdgcn_s_waitcnt(0xC07F | (N << 8)); }
DI float max3(float a, float b, float c) { return __builtin_elementwise_maximum(__builtin_elementwise_maximum(a, b), c); }
template <int IMM> DI void ld16_sc1(u32x4& dst, u32 voff, rsrc_t srd, u32 soff) {
  asm volatile("buffer_load_dwordx4 %0, %1, %2, %3 offen offset:%4 sc1" : "=v"(dst) : "v"(voff), "s"(srd), "s"(soff), "i"(IMM));
}
template <int IMM> DI void ld16_sc1_rw(u32x4& dst, u32 voff, rsrc_t srd, u32 soff) {
  asm volatile("buffer_load_dwordx4 %0, %1, %2, %3 offen offset:%4 sc1" : "+v"(dst) : "v"(voff), "s"(srd), "s"(soff), "i"(IMM));
}
template <int IMM> DI void st16_sc1(const u32x4& src, u32 voff, rsrc_t srd, u32 soff) {
  asm volatile("buffer_store_dwordx4 %0, %1, %2, %3 offen offset:%4 sc1\n\ts_nop 1" :: "v"(src), "v"(voff), "s"(srd), "s"(soff), "i"(IMM) : "memory");
}
template <int IMM> DI void st16_o(const u32x4& src, u32 voff, rsrc_t srd, u32 soff) {
  st16_sc1<IMM>(src, voff, srd, soff);
}
DI void ld4_touch_s(u32& dst, u32 voff, rsrc_t srd, u32 soff) { asm volatile("buffer_load_dword %0, %1, %2, %3 offen" : "+v"(dst) : "v"(voff), "s"(srd), "s"(soff)); }
#define PF_POL ""
DI void ld4_touch(u32& dst, u32 voff, rsrc_t srd) { asm volatile("buffer_load_dword %0, %1, %2, 0 offen" PF_POL : "+v"(dst) : "v"(voff), "s"(srd)); }
DI void keep(u32x4& a) { asm volatile("" : "+v"(a)); }
template <int N> DI void wait_vm(u32x4& a) { asm volatile("s_waitcnt vmcnt(%1)" : "+v"(a) : "i"(N)); }
template <int N> DI void wait_vm(u64& a) { asm volatile("s_waitcnt vmcnt(%1)" : "+v"(a) : "i"(N)); }
DI void ld8_sc1(u64& dst, u32 voff, gu64* base) { asm volatile("global_load_dwordx2 %0, %1, %2 sc1" : "=v"(dst) : "v"(voff), "s"(base)); }
template <int K> DI void flags_again(u64& fl, u32 voff, gu64* base, u64 tag) {
  u32 cnt;
  asm volatile("s_waitcnt vmcnt(%5)\n\tv_cmp_ne_u64_e32 vcc, %4, %0\n\ts_cbranch_vccz 2f\n\ts_mov_b32 %1, 0x40000\n"
               "1:\n\tglobal_load_dwordx2 %0, %2, %3 sc1\n\ts_waitcnt vmcnt(0)\n\tv_cmp_ne_u64_e32 vcc, %4, %0\n\ts_cbranch_vccz 2f\n\t"
               "s_sleep 2\n\ts_sub_u32 %1, %1, 1\n\ts_cmp_lg_u32 %1, 0\n\ts_cbranch_scc1 1b\n2:"
               : "+v"(fl), "=&s"(cnt) : "v"(voff), "s"(base), "s"(tag), "i"(K) : "vcc", "scc", "memory");
}
DI void head_again(u32x4* f, u32 early, u32 fvoff, gu64* base, u64 tag, u32 voff, rsrc_t srd, u32 s0, u32 s1, u32 s2) {
  u32 cnt; u64 fl;
  asm volatile("s_cmp_lg_u32 %[early], 0\n\ts_cbranch_scc1 3f\n\ts_mov_b32 %[cnt], 0x40000\n"
               "1:\n\tglobal_load_dwordx2 %[fl], %[fvoff], %[base] sc1\n\ts_waitcnt vmcnt(0)\n\tv_cmp_ne_u64_e32 vcc, %[tag], %[fl]\n\ts_cbranch_vccz 2f\n\t"
               "s_sleep 2\n\ts_sub_u32 %[cnt], %[cnt], 1\n\ts_cmp_lg_u32 %[cnt], 0\n\ts_cbranch_scc1 1b\n"
               "2:\n\t"
               "buffer_load_dwordx4 %0, %[voff], %[srd], %[s0] offen sc1\n\tbuffer_load_dwordx4 %1, %[voff], %[srd], %[s0] offen offset:1024 sc1\n\t"
               "buffer_load_dwordx4 %2, %[voff], %[srd], %[s0] offen offset:2048 sc1\n\tbuffer_load_dwordx4 %3, %[voff], %[srd], %[s0] offen offset:3072 sc1\n\t"
               "buffer_load_dwordx4 %4, %[voff], %[srd], %[s1] offen sc1\n\tbuffer_load_dwordx4 %5, %[voff], %[srd], %[s1] offen offset:1024 sc1\n\t"
               "buffer_load_dwordx4 %6, %[voff], %[srd], %[s1] offen offset:2048 sc1\n\tbuffer_load_dwordx4 %7, %[voff], %[srd], %[s1] offen offset:3072 sc1\n\t"
               "buffer_load_dwordx4 %8, %[voff], %[srd], %[s2] offen sc1\n\tbuffer_load_dwordx4 %9, %[voff], %[srd], %[s2] offen offset:1024 sc1\n\t"
               "buffer_load_dwordx4 %10, %[voff], %[srd], %[s2] offen offset:2048 sc1\n\tbuffer_load_dwordx4 %11, %[voff], %[srd], %[s2] offen offset:3072 sc1\n\t"
               "s_waitcnt vmcnt(0)\n3:"
               : "+v"(f[0]), "+v"(f[1]), "+v"(f[2]), "+v"(f[3]), "+v"(f[4]), "+v"(f[5]), "+v"(f[6]), "+v"(f[7]), "+v"(f[8]), "+v"(f[9]), "+v"(f[10]), "+v"(f[11]),
                 [cnt] "=&s"(cnt), [fl] "=&v"(fl)
               : [early] "s"(early), [fvoff] "v"(fvoff), [base] "s"(base), [tag] "s"(tag), [voff] "v"(voff), [srd] "s"(srd), [s0] "s"(s0), [s1] "s"(s1), [s2] "s"(s2)
               : "vcc", "scc", "memory");
}
#define OUT_POL 0
DI void st_out(char* ptr, const u32x4& v) {
  if constexpr (OUT_POL == 0) *(GAS u32x4*)ptr = v;
  else if constexpr (OUT_POL == 1) asm volatile("global_store_dwordx4 %0, %1, off sc1\n\ts_nop 1" :: "v"(ptr), "v"(v) : "memory");
  else if constexpr (OUT_POL == 2) asm volatile("global_store_dwordx4 %0, %1, off nt\n\ts_nop 1" :: "v"(ptr), "v"(v) : "memory");
  else asm volatile("global_store_dwordx4 %0, %1, off sc0 sc1\n\ts_nop 1" :: "v"(ptr), "v"(v) : "memory");
}
template <int N> DI void wait_bar() { asm volatile("s_waitcnt vmcnt(%0)\n\ts_barrier" :: "i"(N) : "memory"); }
template <int N> DI void wait_bar_lgkm() {
  asm volatile("" ::: "memory");
  __builtin_amdgcn_s_waitcnt(0x0070 | (N & 15) | ((N >> 4) << 14));
  asm volatile("s_barrier" ::: "memory");
}
DI bf16x8 lds_b128(u32 addr) { return *(const LAS bf16x8*)(size_t)addr; }
DI s16x4 lds_tr(u32 a) { return __builtin_amdgcn_ds_read_tr16_b64_v4i16((LAS s16x4*)(size_t)a); }
DI bf16x8 comb(s16x4 lo, s16x4 hi) { s16x8 v = __builtin_shufflevector(lo, hi, 0, 1, 2, 3, 4, 5, 6, 7); return __builtin_bit_cast(bf16x8, v); }
DI void put_tag(gu64* word, u64 tag) { __hip_atomic_store(word, tag, __ATOMIC_RELAXED, __HIP_MEMORY_SCOPE_AGENT); }
template <int SLEEP = 2> DI void poll_tags(gu64* words, u32 nmask, u32 lane, u64 tag) {
  u32 spins = 0;
  gu64* wp = words + (lane & nmask);
  while (true) {
    const u64 v = __hip_atomic_load(wp, __ATOMIC_RELAXED, __HIP_MEMORY_SCOPE_AGENT);
    if (__builtin_amdgcn_ballot_w64(v != tag) == 0ull) break;
    __builtin_amdgcn_s_sleep(SLEEP);
    if (++spins > (1u << 18)) break;
  }
}

#define KV_NT 1
#define W_NT 0
#define WO_NT 0
#define X_NT 0
#define THROTTLE_IT -1
#define USE_PRIO 1
#define PRIO_QA 2
#define PRIO_QB 3
#define PRIO_AA 1
#define PRIO_AB 2
#define PRIO_OA 3
#define PRIO_OB 0
#define PF_WQB -1
#define PF_WOA 0
#define PF_WOB 0
#define PF_OWN 0
#define PF_END 2
#define PF_START 0
#define PF_A 0
#define PF_B 2
#define QP_SW -1
#define PRIO_QB2 1
#define PF_MID -1
#define PF_MID_X 0
#define PF_MID_BOTH 0
#define PF_LINES 4
#define B_SLEEP 0
#define OP_BAR 15
#define OP_L1 3
#define OP_L2 4
#define OP_FLT 9
#define OP_CNT_ST 12
#define OP_EARLY 1

struct OpPlan {
  int kb[24];
  int ko[24][4];
  int ld[24][2];
  int kf, first_s;
  int put;
};
constexpr int op_pieces(int f) { return f < OP_BAR ? 0 : (5 / (20 - OP_BAR)) + ((f - OP_BAR) < 5 % (20 - OP_BAR) ? 1 : 0); }
constexpr int op_ldpos(int nld, int f) {
  return nld == 1 ? ((f & 3) == 1 && f < 16 ? f >> 2 : -1) : nld == 2 ? ((f & 1) == 1 && f < 16 ? f >> 1 : -1) : -1; }
constexpr int own_wo3(int n, int f) { return n == 0 && (f & 1) && f < 10 ? f >> 1 : -1; }
constexpr int own_ld(int n, int f) { return n == 1 || n == 2 ? ((f & 3) == 1 && f < 16 ? f >> 2 : -1) : -1; }
constexpr int own_st(int n, int f) { return n == 0 ? (f == 4 ? 0 : f == 8 ? 1 : f == 12 ? 2 : f == 14 ? 3 : -1) : n < 3 ? (f == 3 ? 0 : f == 7 ? 1 : f == 11 ? 2 : f == 14 ? 3 : -1) : -1; }
constexpr OpPlan make_plan() {
  OpPlan P{};
  int c = 0, posW[28] = {}, posO[24][4] = {}, posF = 0, posS = 0;
  posW[1] = 4; c = 5;
  c += 5; posW[2] = c - 1;
  for (int q = 0; q < 4; ++q) posO[3][q] = c++;
  c += 12;
  P.first_s = 15 - OP_L2; P.put = -1;
  for (int n = 0; n < 24; ++n) {
    int k = 0; P.ld[n][0] = P.ld[n][1] = -1;
    for (int t = 6; t < 24; ++t) if (t - (t < 15 ? OP_L1 : OP_L2) == n) P.ld[n][k++] = t;
    if (n == P.first_s) P.kf = c - 1 - posF;
    for (int f = 0; f < 20; ++f) {
      if (f % 5 == 0) { const int y = n >= 3 ? c - 1 - posO[n][f / 5] : 63; P.ko[n][f / 5] = y > 63 ? 63 : y; }
      if (f == OP_BAR && n < 23) {
        const int y = c - 1 - posW[n + 1]; P.kb[n] = y > 63 ? 63 : y;
        if (P.put < 0 && n >= 2 && posW[n + 1] > posS) P.put = n;
        if (n == OP_FLT) posF = c++;
      }
      const int lp = op_ldpos(k, f);
      if (lp >= 0) posO[P.ld[n][lp >> 2]][lp & 3] = c++;
      if (own_wo3(n, f) >= 0) posW[3] = c++;
      if (own_ld(n, f) >= 0) posO[3 + n][own_ld(n, f)] = c++;
      if (own_st(n, f) >= 0) posS = c++;
      if (n + 4 < 24) { c += op_pieces(f); if (op_pieces(f)) posW[n + 4] = c - 1; }
    }
  }
  return P;
}
constexpr OpPlan PLAN = make_plan();
static_assert(PLAN.put == 5 && PLAN.put < OP_FLT, "");

__global__ __launch_bounds__(256, 2) void fused_kernel(Params p) {
  __shared__ __attribute__((aligned(1024))) char smem[LDS_BYTES];
  const u32 ldsb = (u32)(size_t)(LAS char*)smem;
  const u32 tid = threadIdx.x, lane = tid & 63u, w = rfl(tid >> 6);
  const u32 r = lane & 31u, hh = lane >> 5;
  const u32 blk = blockIdx.x, role = blk >> 8, idx = blk & 255u, sl = idx >> 3;
  const u32 br = (idx >> 2) & 1u, j8 = idx & 3u, b = sl >> 2, i = sl & 3u, j = 2u * j8 + role, h = 4u * j + i;
  char* const wsb = (char*)p.ws;
  if (USE_PRIO) { if (role == 0) __builtin_amdgcn_s_setprio(PRIO_QA); else __builtin_amdgcn_s_setprio(PRIO_QB); }
  u64 ev;
  { const u32 vz = 0; asm volatile("global_load_dwordx2 %0, %1, %2 sc1" : "=v"(ev) : "v"(vz), "s"(wsb)); }

  const rsrc_t srdW = mk_rsrc((const char*)p.wq[br] + h * (DH * 2), (u32)(DM * HD * 2) - h * (DH * 2));
  const rsrc_t srdX = mk_rsrc((const char*)p.x[br] + (size_t)b * SQ * DM * 2, SQ * DM * 2);
  const rsrc_t srdK = mk_rsrc((const char*)p.kc[br] + (size_t)(b * NH + h) * SK * DH * 2, SK * DH * 2);
  const rsrc_t srdV = mk_rsrc((const char*)p.vc[br] + (size_t)(b * NH + h) * SK * DH * 2, SK * DH * 2);

  u32 voffW[3], voffK[3], voffV[3], voffX[2];
#pragma unroll
  for (int v = 0; v < 3; ++v) {
    const u32 z = 64u * v + lane, rr = z / 24u, ph = z % 24u;
    const u32 cV = ph ^ (((rr >> 1) & 1u) << 2);
    const u32 cK = (ph & ~7u) | ((ph & 7u) ^ (((w & 1u) << 2) | (rr >> 1)));
    voffW[v] = rr * (HD * 2) + cV * 16u;
    voffV[v] = rr * 384u + cV * 16u;
    voffK[v] = rr * 384u + cK * 16u;
  }
#pragma unroll
  for (int v = 0; v < 2; ++v) voffX[v] = (lane >> 3) * (DM * 2) + ((lane & 7u) ^ ((u32)(v << 2) | (lane >> 4))) * 16u;
  const u32 blkb = (lane >> 4) & 1u, q4 = (lane & 15u) >> 2, pp = lane & 3u, e = (q4 >> 1) & 1u;
  const u32 bW0 = ldsb + (8u * hh + q4) * 384u + (4u * e + 2u * blkb + (pp & 1u)) * 16u + 8u * (pp >> 1), bW1 = bW0 ^ 64u;
  const u32 baseV0 = ldsb + 12288u + (4u * hh + q4) * 384u + (4u * e + 2u * blkb + (pp >> 1)) * 16u + 8u * (pp & 1u);
  const u32 baseK0 = ldsb + r * 384u + ((hh ^ ((r >> 1) & 7u)) << 4);
  const u32 baseX0 = ldsb + XOFF + w * 4096u + r * 128u + ((hh ^ ((r >> 1) & 7u)) << 4);

  const u32 c0 = (b + (b >> 2) + 2u * i + (i >> 1)) % 10u;
  auto chunk_of = [&](u32 c) { u32 t = c + c0; return t >= 10u ? t - 10u : t; };
  auto wq_piece = [&](u32 slot, u32 so, int v) { dma16<W_NT>(ldsb + slot * WQS + w * 3072u + v * 1024u, voffW[v], srdW, so); };
  auto wq_so = [&](u32 t) { return (chunk_of(t >> 1) * 64u + (t & 1u) * 32u + 8u * w) * (HD * 2); };
  auto x_piece = [&](u32 par, u32 so, int v) { dma16<X_NT>(ldsb + XOFF + par * 16384u + w * 4096u + v * 1024u, voffX[v & 1], srdX, so + v * 8u * (DM * 2)); };
  auto x_so = [&](u32 c) { return 32u * w * (DM * 2) + chunk_of(c) * 128u; };
  auto k_piece = [&](u32 t, u32 slot_off, int v) { dma16<KV_NT>(ldsb + slot_off + w * 3072u + v * 1024u, voffK[v], srdK, t * 12288u + w * 3072u); };
  auto v_piece = [&](u32 t, u32 slot_off, int v) { dma16<KV_NT>(ldsb + slot_off + 12288u + w * 3072u + v * 1024u, voffV[v], srdV, t * 12288u + w * 3072u); };

  f32x16 acc[6];
#pragma unroll
  for (int t = 0; t < 6; ++t) acc[t] = f32x16{};
  if (role == 1) {
    const u64 t0 = __builtin_amdgcn_s_memrealtime();
    u32 d0 = 0, d1 = 0;
    if (PF_WQB >= 0) {
      while (__builtin_amdgcn_s_memrealtime() - t0 < (u64)(PF_WQB)) __builtin_amdgcn_s_sleep(4);
      const u32 z = 240u * sl + (tid < 240u ? tid : 0u), row = z / 12u, ln = z % 12u;
      const rsrc_t srdPF = mk_rsrc((const char*)p.wq[br] + (size_t)(4u * j) * DH * 2, (u32)(DM * HD * 2) - 4u * j * (DH * 2));
      asm volatile("buffer_load_dword %0, %1, %2, 0 offen" : "=v"(d0) : "v"(row * (HD * 2) + ln * 128u), "s"(srdPF));
      if (PF_WOA == 2) {
        const rsrc_t srdP2 = mk_rsrc((const char*)p.wo[br] + (size_t)(1536u * j8) * DM * 2, 768 * DM * 2);
        asm volatile("buffer_load_dword %0, %1, %2, 0 offen" : "=v"(d1) : "v"((240u * sl + (tid < 240u ? tid : 0u)) * 128u), "s"(srdP2));
      }
    }
    while (__builtin_amdgcn_s_memrealtime() - t0 < (u64)(B_SLEEP)) __builtin_amdgcn_s_sleep(4);
    if (PF_WOA == 1) {
      const rsrc_t srdPF = mk_rsrc((const char*)p.wo[br] + (size_t)(1536u * j8) * DM * 2, 768 * DM * 2);
      asm volatile("buffer_load_dword %0, %1, %2, 0 offen" : "=v"(d1) : "v"((240u * sl + (tid < 240u ? tid : 0u)) * 128u), "s"(srdPF));
    }
    asm volatile("s_waitcnt vmcnt(0)" : "+v"(d0), "+v"(d1) :: "memory");
  }
  {
    const u32 s0 = wq_so(0), s1 = wq_so(1), s2 = wq_so(2), s3 = wq_so(3), x0 = x_so(0), x1 = x_so(1);
#pragma unroll
    for (int v = 0; v < 3; ++v) wq_piece(0, s0, v);
#pragma unroll
    for (int v = 0; v < 4; ++v) x_piece(0, x0, v);
#pragma unroll
    for (int v = 0; v < 3; ++v) wq_piece(1, s1, v);
#pragma unroll
    for (int v = 0; v < 3; ++v) wq_piece(2, s2, v);
#pragma unroll
    for (int v = 0; v < 4; ++v) x_piece(1, x1, v);
#pragma unroll
    for (int v = 0; v < 3; ++v) wq_piece(3, s3, v);
  }
  u32 ps0 = 0, ps1 = 0;
  auto touch_wo = [&]() {
    const rsrc_t srdP = mk_rsrc((const char*)p.wo[br] + (size_t)(1536u * j8 + 768u * role) * DM * 2, 768 * DM * 2);
    ld4_touch(ps0, (240u * sl + (tid < 240u ? tid : 0u)) * 128u, srdP);
  };
  auto touch_wo2 = [&](u32 who) {
    const rsrc_t srdP = mk_rsrc((const char*)p.wo[br] + (size_t)(1536u * j8) * DM * 2, role == who ? 1536 * DM * 2 : 0);
    const u32 vo = (240u * sl + (tid < 240u ? tid : 0u)) * 128u;
    ld4_touch(ps0, vo, srdP); ld4_touch(ps0, vo + 7680u * 128u, srdP);
  };
  if (PF_START == 2) touch_wo();
  if (PF_START == 14) touch_wo2(0);
  if (PF_START == 15) touch_wo2(1);
  const rsrc_t srdT = mk_rsrc((const char*)p.wo[br] + (size_t)(1536u * j8 + 768u * role) * DM * 2, 768 * DM * 2);
  const u32 voffT = lane < PF_LINES ? (240u * sl + 60u * w + lane) * 128u : 0x7fff0000u;
  if (PF_START == 1) {
    const u32 z = 240u * sl + (tid < 240u ? tid : 0u), row = z / 12u, ln = z % 12u;
    const rsrc_t srdP1 = mk_rsrc((const char*)p.wq[br] + (size_t)(4u * j) * DH * 2, (u32)(DM * HD * 2) - 4u * j * (DH * 2));
    asm volatile("buffer_load_dword %0, %1, %2, 0 offen" : "=v"(ps0) : "v"(row * (HD * 2) + ln * 128u), "s"(srdP1));
    const rsrc_t srdP2 = mk_rsrc((const char*)p.x[br], NB * SQ * DM * 2);
    asm volatile("buffer_load_dword %0, %1, %2, 0 offen" : "=v"(ps1) : "v"(((role * 32u + sl) * 160u + (tid < 160u ? tid : 0u)) * 128u), "s"(srdP2));
  }
  asm volatile("s_waitcnt vmcnt(%1)" : "+v"(ev) : "i"(PF_START == 1 ? 22 : 20) : "memory");
  const u64 tag = (((u64)rfl((u32)(ev >> 32)) << 32) | rfl((u32)ev)) + 1ull;
  asm volatile("s_waitcnt vmcnt(%0)\n\ts_barrier" :: "i"(PF_START == 1 ? 15 : 13) : "memory");
  if (tid == 0) put_tag((gu64*)(wsb + WS_ENTER) + blk, tag);
  auto rdA = [&](int slot, int ks, int m, int hi) {
    return lds_tr(((m & 1) ? bW1 : bW0) + slot * WQS + ks * (16 * 384) + (m >> 1) * 128 + hi * (4 * 384)); };
  auto rdX = [&](int par, int xks) { return lds_b128((baseX0 ^ (u32)(xks << 5)) + par * 16384); };
  s16x4 nlo[6], nhi[6];
  bf16x8 Xn;
  auto qp_ks0 = [&](auto Sc, const s16x4 (&lo)[6], const s16x4 (&hi)[6], const bf16x8& X) {
    constexpr int S = decltype(Sc)::value, PAR = S >> 1, XK = (S & 1) * 2;
    MF(acc[0], comb(lo[0], hi[0]), X); nlo[0] = rdA(S, 1, 0, 0); nhi[0] = rdA(S, 1, 0, 1); nlo[1] = rdA(S, 1, 1, 0); PIN();
    MF(acc[1], comb(lo[1], hi[1]), X); nhi[1] = rdA(S, 1, 1, 1); nlo[2] = rdA(S, 1, 2, 0); nhi[2] = rdA(S, 1, 2, 1); PIN();
    MF(acc[2], comb(lo[2], hi[2]), X); nlo[3] = rdA(S, 1, 3, 0); nhi[3] = rdA(S, 1, 3, 1); nlo[4] = rdA(S, 1, 4, 0); PIN();
    MF(acc[3], comb(lo[3], hi[3]), X); nhi[4] = rdA(S, 1, 4, 1); nlo[5] = rdA(S, 1, 5, 0); nhi[5] = rdA(S, 1, 5, 1); PIN();
    MF(acc[4], comb(lo[4], hi[4]), X); Xn = rdX(PAR, XK + 1); PIN();
    MF(acc[5], comb(lo[5], hi[5]), X); PIN();
  };
  {
    s16x4 Alo[6], Ahi[6];
#pragma unroll
    for (int m = 0; m < 6; ++m) { Alo[m] = rdA(0, 0, m, 0); Ahi[m] = rdA(0, 0, m, 1); }
    const bf16x8 X = rdX(0, 0);
    PIN();
    qp_ks0(std::integral_constant<int, 0>{}, Alo, Ahi, X);
  }
  auto qp_tile = [&](auto TTc, auto LASTc, u32 per) {
    constexpr int TT = decltype(TTc)::value; constexpr bool LAST = decltype(LASTc)::value;
    constexpr int PAR = TT >> 1;
    if constexpr (!LAST) { if constexpr (TT & 1) wait_bar_lgkm<6>(); else wait_bar_lgkm<10>(); }
    else { if constexpr (TT == 0) wait_bar_lgkm<10>(); else if constexpr (TT < 3) wait_bar_lgkm<6>(); else wait_bar_lgkm<63>(); }
    PIN();
    const u32 tn = 4u * per + TT + 4u;
    u32 sw = 0, sx = 0;
    if constexpr (!LAST) { sw = wq_so(tn); if constexpr (TT & 1) sx = x_so(tn >> 1); }
    constexpr int NS = (TT + 1) & 3, NPAR = NS >> 1, NXK = (NS & 1) * 2;
    constexpr bool MORE = !(LAST && TT == 3);
    s16x4 Alo[6], Ahi[6]; bf16x8 X;
#pragma unroll
    for (int m = 0; m < 6; ++m) {
      MF(acc[m], comb(nlo[m], nhi[m]), Xn);
      if constexpr (MORE) { Alo[m] = rdA(NS, 0, m, 0); Ahi[m] = rdA(NS, 0, m, 1); if (m == 0) X = rdX(NPAR, NXK); }
      if constexpr (!LAST) {
        if constexpr (TT & 1) {
          if (m == 0) { x_piece(PAR, sx, 0); } if (m == 1) { x_piece(PAR, sx, 1); x_piece(PAR, sx, 2); } if (m == 2) x_piece(PAR, sx, 3);
          if (m >= 3) wq_piece(TT, sw, m - 3);
        } else { if (m & 1) wq_piece(TT, sw, m >> 1); }
      } else {
        if constexpr (TT == 0) { if (m & 1) k_piece(0, 0, m >> 1); }
        if constexpr (TT == 1) { if (m & 1) v_piece(0, 0, m >> 1); }
        if constexpr (TT == 2) { if (m < 3) k_piece(1, SLOT, m); else k_piece(2, 2 * SLOT, m - 3); }
        if constexpr (TT == 3) { if (m & 1) v_piece(1, SLOT, m >> 1); }
      }
      PIN();
    }
    if constexpr (!LAST && PF_START == 9) { if (PF_LINES == 4 || 4u * per + TT >= 16 - 60 / PF_LINES) ld4_touch_s(ps0, voffT, srdT, (4u * per + TT - (PF_LINES == 4 ? 0 : 16 - 60 / PF_LINES)) * (PF_LINES * 128)); PIN(); }
    if constexpr (MORE) qp_ks0(std::integral_constant<int, NS>{}, Alo, Ahi, X);
  };
  using std::integral_constant;
#define IC(n) integral_constant<int, n>{}
#define BC(n) integral_constant<bool, n>{}
  for (u32 per = 0; per < 4; ++per) { if (QP_SW >= 0 && per == QP_SW && role == 1) __builtin_amdgcn_s_setprio(PRIO_QB2); if (PF_START >= 3 && PF_START < 8 && per == PF_START - 3) touch_wo(); if (PF_START == 8 && per == (role == 0 ? PF_A : PF_B)) touch_wo(); if (PF_START >= 10 && PF_START < 14 && per == PF_START - 10) touch_wo2(1); qp_tile(IC(0), BC(false), per); qp_tile(IC(1), BC(false), per); qp_tile(IC(2), BC(false), per); qp_tile(IC(3), BC(false), per); }
  if (QP_SW == 4 && role == 1) __builtin_amdgcn_s_setprio(PRIO_QB2);
  qp_tile(IC(0), BC(true), 4); qp_tile(IC(1), BC(true), 4); qp_tile(IC(2), BC(true), 4); qp_tile(IC(3), BC(true), 4);
  if (PF_START) asm volatile("" : "+v"(ps0), "+v"(ps1));
  constexpr float CSC = 0.07216878364870322f * 1.4426950408889634f;
  bf16x8 qf[12];
#pragma unroll
  for (int t = 0; t < 6; ++t) { qf[2 * t] = pack8(acc[t], 0, CSC); qf[2 * t + 1] = pack8(acc[t], 8, CSC); }
  if (PF_WOA >= 3 && role == 1) {
    const rsrc_t srdPF = mk_rsrc((const char*)p.wo[br] + (size_t)(1536u * j8) * DM * 2, 1536 * DM * 2);
    pf4<false>(ldsb + 3 * SLOT + w * 256u, (240u * sl + (tid < 240u ? tid : 0u)) * 128u, srdPF);
    if (PF_WOA == 4) pf4<false>(ldsb + 3 * SLOT + 1024u + w * 256u, (7680u + 240u * sl + (tid < 240u ? tid : 0u)) * 128u, srdPF);
  }
  if (role == 1 && w == 0 && THROTTLE_IT >= 0) poll_tags((gu64*)(wsb + WS_FA + idx * 64u), 0u, lane, tag);
  if (USE_PRIO) { if (role == 0) __builtin_amdgcn_s_setprio(PRIO_AA); else __builtin_amdgcn_s_setprio(PRIO_AB); }

  const rsrc_t srdO = mk_rsrc((const char*)p.wo[br] + ((size_t)(1536u * j8) * DM + 160u * i) * 2, role == 0 ? (u32)(HD * DM * 2) - (1536u * j8 * DM + 160u * i) * 2u : 0u);
  u32 voffO[5];
#pragma unroll
  for (int v = 0; v < 5; ++v) { const u32 z = 64u * v + lane; voffO[v] = (z / 20u) * (DM * 2) + (z % 20u) * 16u; }
  auto wo_so = [&](u32 half, u32 n) { u32 t = n + 3u * i; t = t >= 12u ? t - 12u : t; return ((half * 12u + t) * 64u + 16u * w) * (DM * 2); };
  auto wo_piece = [&](int n, u32 so, int v) { dma16<WO_NT>(ldsb + ((n + 2) & 3) * WOS + w * 5120u + v * 1024u, voffO[v], srdO, so); };

  constexpr float THR = 8.0f;
  f32x16 o[6];
#pragma unroll
  for (int t = 0; t < 6; ++t) o[t] = f32x16{};
  wait_bar<12>();
  u32 kb[4], vb[2];
#pragma unroll
  for (int q = 0; q < 4; ++q) kb[q] = baseK0 ^ (u32)(q << 5);
  vb[0] = baseV0; vb[1] = baseV0 ^ 64u;
  auto rdK = [&](int slot, int st) { return lds_b128(kb[st & 3] + slot * SLOT + (st >> 2) * 128); };
  struct VF { s16x4 lo, hi; };
  auto rdV = [&](int slot, int n) { const int t = n % 6, hf = n / 6; const u32 a = vb[t & 1] + slot * SLOT + (t >> 1) * 128 + hf * (16 * 384); VF f; f.lo = lds_tr(a); f.hi = lds_tr(a + 8 * 384); return f; };
  const u32 dmaB = ldsb + w * 3072u;
  const rsrc_t srdV2 = mk_rsrc((const char*)p.vc[br] + (size_t)(b * NH + h) * SK * DH * 2 - 2 * 12288, SK * DH * 2 + 2 * 12288);
  u32 soK = 3 * 12288u + w * 3072u;
  f32x16 sa, sb, negm;
  float l = 0.f;
  bf16x8 kw[4];
  {
    bf16x8 kf[12];
#pragma unroll
    for (int n = 0; n < 6; ++n) kf[n] = rdK(0, n);
    PIN();
    sa = f32x16{};
#pragma unroll
    for (int n = 0; n < 12; ++n) { MF(sa, kf[n], qf[n]); if (n < 6) kf[n + 6] = rdK(0, n + 6); PIN(); }
    wait_bar_lgkm<6>();
#pragma unroll
    for (int v = 0; v < 3; ++v) dma16<KV_NT>(dmaB + v * 1024u, voffK[v], srdK, soK);
    soK += 12288u;
#pragma unroll
    for (int n = 0; n < 4; ++n) kw[n] = rdK(1, n);
    float pm = max3(sa[0], sa[1], sa[2]);
#pragma unroll
    for (int k = 3; k < 15; k += 2) pm = max3(pm, sa[k], sa[k + 1]);
    pm = __builtin_elementwise_maximum(pm, sa[15]);
    { auto rr = __builtin_amdgcn_permlane32_swap(__float_as_uint(pm), __float_as_uint(pm), false, false); pm = __builtin_elementwise_maximum(__uint_as_float(rr[0]), __uint_as_float(rr[1])); }
#pragma unroll
    for (int k = 0; k < 16; ++k) { sa[k] -= pm; negm[k] = -pm; }
    asm volatile("" : "+v"(negm));
    wait_lgkm<2>();
    PIN();
  }
  gu64* const FO = (gu64*)(wsb + WS_FO + (((br * NB + b) * 8u + j) * 4u + w) * 64u);
  u64 flg;
  u32 pmid = 0;
  auto att_body = [&](auto MODEc, auto S3c, f32x16& sc, f32x16& sn) {
    constexpr int MODE = decltype(MODEc)::value;
    constexpr int S0 = decltype(S3c)::value, S1 = (S0 + 1) % 3, S2 = (S0 + 2) % 3;
    constexpr bool QK = MODE < 15, KNEXT = MODE < 14, DV = MODE < 14, DK = MODE == 0, WO = MODE >= 14;
    u32 pw[8];
    auto E = [&](int k) { sc[k] = __builtin_amdgcn_exp2f(sc[k]); };
    auto C = [&](int q) { pw[q] = cvtpk(sc[2 * q], sc[2 * q + 1]); };
    auto A = [&](int k) { l += sc[k]; };
    bf16x8 kf[12];
    VF vf[12];
    u32 sow = 0;
    if constexpr (WO) sow = wo_so(role, MODE - 14);
    auto D0 = [&](int q) {
      if constexpr (WO) { if (q < 5) set_m0(ldsb + w * 5120u, ((MODE - 14 + 2) & 3) * WOS + q * 1024); }
      else { if (q < 3) { if (DV) set_m0(dmaB, S2 * SLOT + 12288 + q * 1024); } else { if (DK) set_m0(dmaB, S1 * SLOT + (q - 3) * 1024); } }
    };
    auto D1 = [&](int q) {
      if constexpr (WO) { if (q < 5) dma_go<WO_NT>(voffO[q], srdO, sow); }
      else { if (q < 3) { if (DV) dma_go<KV_NT>(voffV[q], srdV2, soK); } else { if (DK) dma_go<KV_NT>(voffK[q - 3], srdK, soK); } }
    };
    float pm = 0.f;
    auto M = [&](int q) { if constexpr (QK) { if (q == 0) pm = max3(sn[0], sn[1], sn[2]); else if (q < 7) pm = max3(pm, sn[2 * q + 1], sn[2 * q + 2]); else pm = __builtin_elementwise_maximum(pm, sn[15]); } };
#pragma unroll
    for (int n = 0; n < 4; ++n) kf[n] = kw[n];
#pragma unroll
    for (int n = 0; n < 12; ++n) {
      if constexpr (QK) {
        if (n == 0) sn = MFMA(kf[0], qf[0], negm); else sn = MFMA(kf[n], qf[n], sn);
        asm volatile("" : "+v"(sn));
        if (n < 8) kf[n + 4] = rdK(S1, n + 4);
      }
      if (n < 8) {
        if ((n & 1) == 0) { E(n); E(n + 1); }
        else { C(n >> 1); A(n - 1); A(n); if constexpr (QK) wait_lgkm<2>(); }
      }
      if (n == 8) { E(8); E(9); }
      if (n == 9) {
        if constexpr (MODE <= 12) wait_bar_lgkm<6>(); else if constexpr (MODE <= 14) wait_bar_lgkm<3>(); else wait_bar_lgkm<5>();
        if constexpr (MODE == PF_MID) { if (PF_MID_BOTH || role == 0) ld4_touch(pmid, (240u * sl + (tid < 240u ? tid : 0u)) * 128u, mk_rsrc((const char*)p.wo[br] + (size_t)(1536u * j8 + 768u * (role ^ PF_MID_X)) * DM * 2, 768 * DM * 2)); }
        if constexpr (MODE == 15) ld8_sc1(flg, (lane & 3u) * 8u, FO + 32);
        if constexpr (MODE == 15 && (PF_END == 1 || PF_END == 2))
          pf4<PF_END == 1>(ldsb + SLOT + w * 256u, (240u * sl + (tid < 240u ? tid : 0u)) * 128u, mk_rsrc((const char*)p.wo[br] + (size_t)(1536u * j8 + 768u * role) * DM * 2, 768 * DM * 2));
        if constexpr (MODE == 15 && PF_END == 6)
          pf4<false>(ldsb + SLOT + w * 256u, (240u * sl + (tid < 240u ? tid : 0u)) * 128u, mk_rsrc((const char*)p.wo[br] + (size_t)(1536u * j8 + 768u * (role ^ 1u)) * DM * 2, 768 * DM * 2));
        if constexpr (MODE == 15 && PF_END == 7)
          pf4<false>(ldsb + SLOT + w * 256u, (240u * sl + (tid < 240u ? tid : 0u)) * 128u, mk_rsrc((const char*)p.wo[br] + (size_t)(1536u * j8) * DM * 2, role == 1 ? 768 * DM * 2 : 0));
        if constexpr (MODE == 15 && PF_END == 5)
          pf4<false>(ldsb + SLOT + w * 256u, (240u * sl + (tid < 240u ? tid : 0u)) * 128u, mk_rsrc((const char*)p.wo[br] + (size_t)(1536u * j8 + 768u * role) * DM * 2, role == 1 ? 768 * DM * 2 : 0));
        if constexpr (MODE == 15 && PF_END == 4) {
          const rsrc_t srdPF = mk_rsrc((const char*)p.wo[br] + (size_t)(1536u * j8) * DM * 2, role == 1 ? 1536 * DM * 2 : 0);
          pf4<false>(ldsb + SLOT + w * 256u, (240u * sl + (tid < 240u ? tid : 0u)) * 128u, srdPF);
          pf4<false>(ldsb + SLOT + 1024u + w * 256u, (7680u + 240u * sl + (tid < 240u ? tid : 0u)) * 128u, srdPF);
        }
        if constexpr (MODE == 15 && PF_END == 3) {
          const rsrc_t srdPF = mk_rsrc((const char*)p.wo[br] + (size_t)(1536u * j8) * DM * 2, role == 0 ? 1536 * DM * 2 : 0);
          pf4<false>(ldsb + SLOT + w * 256u, (7680u + 240u * sl + (tid < 240u ? tid : 0u)) * 128u, srdPF);
          pf4<false>(ldsb + SLOT + 1024u + w * 256u, (240u * sl + (tid < 240u ? tid : 0u)) * 128u, srdPF);
        }
        vf[0] = rdV(S0, 0); vf[1] = rdV(S0, 1);
      }
      if (n == 10) { vf[2] = rdV(S0, 2); D0(0); C(4); E(10); }
      if (n == 11) { vf[3] = rdV(S0, 3); D1(0); E(11); wait_lgkm<4>(); }
      PIN();
    }
    const bf16x8 pf0 = __builtin_bit_cast(bf16x8, u32x4{pw[0], pw[1], pw[2], pw[3]});
    bool resc = false;
#pragma unroll
    for (int n = 0; n < 12; ++n) {
      if (n < 6) { MF(o[n], comb(vf[n].lo, vf[n].hi), pf0); }
      else { const bf16x8 pf1 = __builtin_bit_cast(bf16x8, u32x4{pw[4], pw[5], pw[6], pw[7]}); MF(o[n - 6], comb(vf[n].lo, vf[n].hi), pf1); }
      if (n < 8) vf[n + 4] = rdV(S0, n + 4);
      else if constexpr (KNEXT) kw[n - 8] = rdK(S2, n - 8);
      if (n == 0) { D0(1); C(5); E(12); }
      if (n == 1) { D1(1); E(13); wait_lgkm<4>(); }
      if (n == 2) { D0(2); C(6); E(14); }
      if (n == 3) { D1(2); E(15); wait_lgkm<4>(); }
      if (n == 4) { D0(3); C(7); M(0); }
      if (n == 5) { D1(3); M(1); wait_lgkm<4>(); }
      if (n == 6) { D0(4); M(2); M(3); }
      if (n == 7) { D1(4); M(4); A(8); wait_lgkm<4>(); }
      if (n == 8) { D0(5); M(5); M(6); A(9); }
      if (n == 9) { D1(5); M(7); A(10); if constexpr (KNEXT) wait_lgkm<2>(); else wait_lgkm<0>(); }
      if (n == 10) { if constexpr (QK) resc = __builtin_amdgcn_ballot_w64(pm > THR) != 0ull; A(11); A(12); A(13); }
      if (n == 11) { A(14); A(15); soK += 12288u; if constexpr (KNEXT) wait_lgkm<2>(); }
      PIN();
    }
    if constexpr (QK) {
      if (__builtin_expect(resc, 0)) {
        auto rr = __builtin_amdgcn_permlane32_swap(__float_as_uint(pm), __float_as_uint(pm), false, false);
        const float dl = __builtin_elementwise_maximum(__builtin_elementwise_maximum(__uint_as_float(rr[0]), __uint_as_float(rr[1])), 0.f);
        const float al = __builtin_amdgcn_exp2f(-dl);
        l *= al;
#pragma unroll
        for (int t = 0; t < 6; ++t)
#pragma unroll
          for (int k = 0; k < 16; ++k) o[t][k] *= al;
#pragma unroll
        for (int k = 0; k < 16; ++k) { sn[k] -= dl; negm[k] -= dl; }
      }
    }
  };
  for (u32 it = 0; it < 12; it += 6) {
    att_body(IC(0), IC(0), sa, sb); att_body(IC(0), IC(1), sb, sa);
    att_body(IC(0), IC(2), sa, sb); att_body(IC(0), IC(0), sb, sa);
    if (role == 0 && it + 4 == THROTTLE_IT && tid == 0) put_tag((gu64*)(wsb + WS_FA + idx * 64u), tag);
    att_body(IC(0), IC(1), sa, sb); att_body(IC(0), IC(2), sb, sa);
    if (role == 0 && it + 6 == THROTTLE_IT && tid == 0) put_tag((gu64*)(wsb + WS_FA + idx * 64u), tag);
  }
  att_body(IC(12), IC(0), sa, sb); att_body(IC(13), IC(1), sb, sa);
  att_body(IC(14), IC(2), sa, sb); att_body(IC(15), IC(0), sb, sa);
  if (USE_PRIO) { if (role == 0) __builtin_amdgcn_s_setprio(PRIO_OA); else __builtin_amdgcn_s_setprio(PRIO_OB); }
  const rsrc_t srdOb = mk_rsrc(wsb + WS_O + (size_t)((br * NB + b) * NH) * 4 * 12288, NH * 4 * 12288);
  const u32 voffL = lane * 16u;
  auto wo_son = [&](int n) { return n < 3 ? wo_so(0u, n) : n < 15 ? wo_so(1u, n - 3) : wo_so(0u, n - 12); };
  u32x4 of[24];
  auto o_so = [&](int n) {
    const u32 head = n < 15 ? 4u * j + 4u + ((i + (u32)((n - 3) / 3)) & 3u) : 4u * j + ((i + 1u + (u32)((n - 15) / 3)) & 3u);
    return (head * 4u + w) * 12288u + (u32)(n % 3) * 4096u; };
  auto ld_frag = [&](auto Nc, auto Qc, rsrc_t srd, u32 so) {
    constexpr int N = decltype(Nc)::value, Q = decltype(Qc)::value;
    ld16_sc1<Q * 1024>(of[4 * ((N - 3) % 6) + Q], voffL, srd, so); };
  auto ld_tile = [&](auto Nc, rsrc_t srd) { const u32 so = o_so(decltype(Nc)::value);
    ld_frag(Nc, IC(0), srd, so); ld_frag(Nc, IC(1), srd, so); ld_frag(Nc, IC(2), srd, so); ld_frag(Nc, IC(3), srd, so); };
  const u32 baseO = ldsb + (4u * hh + q4) * 320u + 32u * blkb + 16u * (pp & 1u) + 8u * (pp >> 1);
  auto rdO = [&](int n, int f, int hi) { return lds_tr(baseO + ((n + 2) & 3) * WOS + (f / 5) * (16 * 320) + (f % 5) * 64 + hi * (8 * 320)); };
  s16x4 wlo[5], whi[5];
  asm volatile("s_waitcnt vmcnt(%1)\n\ts_barrier" : "+v"(flg) : "i"(PF_END == 1 || PF_END == 2 || PF_END == 5 || PF_END == 6 || PF_END == 7 ? 6 : PF_END >= 3 ? 7 : 5) : "memory");
  if (PF_MID >= 0) asm volatile("" : "+v"(pmid));
  { auto rr = __builtin_amdgcn_permlane32_swap(__float_as_uint(l), __float_as_uint(l), false, false); l = __uint_as_float(rr[0]) + __uint_as_float(rr[1]); }
  const float inv = 1.0f / l;
  const u32 soOwn = (h * 4u + w) * 12288u;
  u32x4 own[12];
  auto pk_own = [&](int q) { own[q] = __builtin_bit_cast(u32x4, pack8(o[q >> 1], (q & 1) * 8, inv)); };
  auto st_own = [&](auto Qc) { constexpr int Q = decltype(Qc)::value; st16_o<(Q & 3) * 1024>(own[Q], voffL, srdOb, soOwn + (Q >> 2) * 4096u); };
  const u32 early = rfl(OP_EARLY && role == 0 && __builtin_amdgcn_ballot_w64(flg != tag) == 0ull ? 1u : 0u);
  const rsrc_t srdE = mk_rsrc(wsb + WS_O + (size_t)((br * NB + b) * NH) * 4 * 12288, early ? NH * 4 * 12288 : 0);
  if (role == 0) {
    const u32 so = wo_son(2);
#pragma unroll
    for (int v = 0; v < 5; ++v) wo_piece(2, so, v);
    ld_tile(IC(3), srdE);
  }
  PIN();
  {
    const rsrc_t srdS = mk_rsrc(wsb + WS_O + (size_t)((br * NB + b) * NH) * 4 * 12288, role == 1 ? NH * 4 * 12288 : 0);
    auto ps = [&](auto Qc) { constexpr int Q = decltype(Qc)::value; pk_own(Q); st16_o<(Q & 3) * 1024>(own[Q], voffL, srdS, soOwn + (Q >> 2) * 4096u); };
    ps(IC(0)); ps(IC(1)); ps(IC(2)); ps(IC(3)); ps(IC(4)); ps(IC(5)); ps(IC(6)); ps(IC(7)); ps(IC(8)); ps(IC(9)); ps(IC(10)); ps(IC(11));
  }
  if (role == 1) {
    asm volatile("s_waitcnt vmcnt(0)" ::: "memory");
    if (lane == 0) put_tag(FO + i, tag);
    return;
  }

  f32x16 oa[5];
#pragma unroll
  for (int t = 0; t < 5; ++t) oa[t] = f32x16{};
#pragma unroll
  for (int f = 0; f < 5; ++f) { wlo[f] = rdO(0, f, 0); whi[f] = rdO(0, f, 1); }
  u64 fls;
  PIN();
  auto lane_now = [&]() { u32 t = voffL; asm volatile("" : "+v"(t)); return t >> 4; };
  auto op_tile = [&](auto Nc) {
    constexpr int N = decltype(Nc)::value;
    constexpr int B0 = N < 3 ? 0 : 4 * ((N - 3) % 6);
    constexpr int L0 = PLAN.ld[N][0], L1 = PLAN.ld[N][1], NL = (L0 >= 0) + (L1 >= 0);
    if constexpr (N == 3) {
      head_again(of, early, (lane_now() & 3u) * 8u, FO + 32, tag, voffL, srdOb, o_so(3), o_so(4), o_so(5));
    }
    if constexpr (N == PLAN.first_s) {
      flags_again<PLAN.kf>(fls, (lane_now() & 3u) * 8u, FO, tag);
    }
    u32 so = 0, s0 = 0, s1 = 0;
    if constexpr (L0 >= 0) s0 = o_so(L0);
    if constexpr (L1 >= 0) s1 = o_so(L1);
    auto step = [&](auto Fc) {
      constexpr int f = decltype(Fc)::value;
      if constexpr (N >= 3 && f % 5 == 0 && PLAN.ko[N][f / 5] < 63) wait_vm<PLAN.ko[N][f / 5]>(of[B0 + f / 5]);
      const u32x4 bfr = N < 3 ? own[4 * (N < 3 ? N : 0) + f / 5] : of[B0 + f / 5];
      MF(oa[f % 5], comb(wlo[f % 5], whi[f % 5]), __builtin_bit_cast(bf16x8, bfr));
      if constexpr (f == OP_BAR && N < 23) {
        wait_bar_lgkm<PLAN.kb[N]>();
        if constexpr (N == PLAN.put) { if (voffL == 0) put_tag(FO + i, tag); }
        if constexpr (N == OP_FLT) ld8_sc1(fls, (lane_now() & 3u) * 8u, FO);

        if constexpr (N + 4 < 24) so = wo_son(N + 4);
      }
      if constexpr (f < 15) { wlo[f % 5] = rdO(N, f + 5, 0); whi[f % 5] = rdO(N, f + 5, 1); }
      else if constexpr (N < 23 && f >= OP_BAR) {
#pragma unroll
        for (int q = (f == OP_BAR ? 0 : f - 15); q <= f - 15; ++q) { wlo[q] = rdO(N + 1, q, 0); whi[q] = rdO(N + 1, q, 1); }
      }
      constexpr int lp = op_ldpos(NL, f);
      if constexpr (lp >= 0) {
        if constexpr (lp < 4) ld_frag(IC(L0 < 3 ? 3 : L0), IC(lp & 3), srdOb, s0); else ld_frag(IC(L1 < 3 ? 3 : L1), IC(lp & 3), srdOb, s1); }
      if constexpr (N < 3) {
        if constexpr (own_wo3(N, f) >= 0) wo_piece(3, wo_son(3), own_wo3(N, f));
        if constexpr (own_ld(N, f) >= 0) ld_frag(IC(3 + N), IC(own_ld(N, f) & 3), srdE, o_so(3 + N));
        if constexpr (own_st(N, f) >= 0) st_own(IC(4 * N + (own_st(N, f) & 3)));
      }
      if constexpr (N + 4 < 24 && f >= OP_BAR) {
        constexpr int done = [] { int d = 0; for (int x = 0; x < f; ++x) d += op_pieces(x); return d; }();
#pragma unroll
        for (int v = 0; v < op_pieces(f); ++v) {
          wo_piece(N + 4, so, done + v);
        }
      }
      PIN();
    };
    step(IC(0)); step(IC(1)); step(IC(2)); step(IC(3)); step(IC(4)); step(IC(5)); step(IC(6)); step(IC(7)); step(IC(8)); step(IC(9));
    step(IC(10)); step(IC(11)); step(IC(12)); step(IC(13)); step(IC(14)); step(IC(15)); step(IC(16)); step(IC(17)); step(IC(18)); step(IC(19));
  };
  op_tile(IC(0)); op_tile(IC(1)); op_tile(IC(2)); op_tile(IC(3)); op_tile(IC(4)); op_tile(IC(5)); op_tile(IC(6)); op_tile(IC(7));
  op_tile(IC(8)); op_tile(IC(9)); op_tile(IC(10)); op_tile(IC(11));
  op_tile(IC(12)); op_tile(IC(13)); op_tile(IC(14)); op_tile(IC(15)); op_tile(IC(16)); op_tile(IC(17)); op_tile(IC(18)); op_tile(IC(19));
  op_tile(IC(20)); op_tile(IC(21)); op_tile(IC(22)); op_tile(IC(23));
  const rsrc_t srdP = mk_rsrc(wsb + WS_P + (size_t)((((br * NB + b) * 4u + i) * 4u + w) * 40u) * 1024u, 40 * 1024);
#pragma unroll
  for (int u = 0; u < 10; ++u) st16_sc1<0>(__builtin_bit_cast(u32x4, pack8n(oa[u >> 1], (u & 1) * 8)), voffL, srdP, (u * 4u + j8) * 1024u);
  asm volatile("s_waitcnt vmcnt(0)" ::: "memory");
  gu64* const FP = (gu64*)(wsb + WS_FP + (((br * NB + b) * 4u + i) * 4u + w) * 64u);
  if (voffL == 0) put_tag(FP + j8, tag);
  const u32 ln = lane_now();
  poll_tags(FP, 3u, ln, tag);
  asm volatile("" ::: "memory");
  {
    u32x4 pv[12];
    const u32 so = j8 * 8192u, s2 = (8u + (j8 & 1u)) * 4096u;
    ld16_sc1<0>(pv[0], voffL, srdP, so); ld16_sc1<1024>(pv[1], voffL, srdP, so); ld16_sc1<2048>(pv[2], voffL, srdP, so); ld16_sc1<3072>(pv[3], voffL, srdP, so);
    ld16_sc1<0>(pv[4], voffL, srdP, so + 4096u); ld16_sc1<1024>(pv[5], voffL, srdP, so + 4096u); ld16_sc1<2048>(pv[6], voffL, srdP, so + 4096u); ld16_sc1<3072>(pv[7], voffL, srdP, so + 4096u);
    const u32 vo2 = j8 < 2u ? voffL : 0x7fff0000u;
    ld16_sc1<0>(pv[8], vo2, srdP, s2); ld16_sc1<1024>(pv[9], vo2, srdP, s2); ld16_sc1<2048>(pv[10], vo2, srdP, s2); ld16_sc1<3072>(pv[11], vo2, srdP, s2);
    asm volatile("s_waitcnt vmcnt(0)" : "+v"(pv[0]), "+v"(pv[1]), "+v"(pv[2]), "+v"(pv[3]), "+v"(pv[4]), "+v"(pv[5]), "+v"(pv[6]), "+v"(pv[7]),
                 "+v"(pv[8]), "+v"(pv[9]), "+v"(pv[10]), "+v"(pv[11]));
    auto red = [&](int q) {
      u32x4 ov;
#pragma unroll
      for (int k = 0; k < 4; ++k) {
        float a = __uint_as_float(pv[q][k] << 16), c = __uint_as_float(pv[q][k] & 0xffff0000u);
#pragma unroll
        for (int jj = 1; jj < 4; ++jj) { a += __uint_as_float(pv[q + jj][k] << 16); c += __uint_as_float(pv[q + jj][k] & 0xffff0000u); }
        ov[k] = cvtpk(a, c);
      }
      return ov;
    };
    char* const ob = (char*)p.out + ((size_t)(b * SQ + 32u * w) * (2 * DM) + br * DM + 160u * i) * 2;
    const u32x4 e0 = red(0), e1 = red(4);
    u32x4 f0, f1;
#pragma unroll
    for (int k = 0; k < 4; ++k) { auto rr = __builtin_amdgcn_permlane16_swap(e0[k], e1[k], false, false); f0[k] = rr[0]; f1[k] = rr[1]; }
    char* const orow = ob + (size_t)(ln & 15u) * (4 * DM) + (32u * j8 + 16u * ((ln >> 4) & 1u) + 8u * (ln >> 5)) * 2;
    st_out(orow, f0);
    st_out(orow + 16 * 4 * DM, f1);
    if (j8 < 2u) st_out(ob + (size_t)(ln & 31u) * (4 * DM) + (16u * (8u + j8) + 8u * (ln >> 5)) * 2, red(8));
  }
  if (blk == 0 && w == 0) {
    u32 spins = 0;
    while (spins < (1u << 20)) {
      bool ok = true;
#pragma unroll
      for (int q = 0; q < 8; ++q) ok &= __hip_atomic_load((gu64*)(wsb + WS_ENTER) + q * 64 + ln, __ATOMIC_RELAXED, __HIP_MEMORY_SCOPE_AGENT) == tag;
      if (__builtin_amdgcn_ballot_w64(!ok) == 0ull) break;
      __builtin_amdgcn_s_sleep(8);
      if (++spins > (1u << 16)) break;
    }
    if (ln == 0) put_tag((gu64*)(wsb + WS_E), tag);
  }
}

extern "C" void kernel_launch(void* const* d_in, const int* in_sizes, int n_in, void* d_out, int out_size, void* d_ws, size_t ws_size, hipStream_t stream) {
  Params p;
  for (int br = 0; br < 2; ++br) {
    p.x[br] = d_in[br * 5 + 0]; p.kc[br] = d_in[br * 5 + 1]; p.vc[br] = d_in[br * 5 + 2]; p.wq[br] = d_in[br * 5 + 3]; p.wo[br] = d_in[br * 5 + 4];
  }
  p.out = d_out; p.ws = d_ws;
  hipLaunchKernelGGL(fused_kernel, dim3(512), dim3(256), 0, stream, p);
}
```

```cpp
#include <hip/hip_runtime.h>
#include <stdint.h>

typedef unsigned u32;
typedef unsigned long long u64;
typedef __bf16 bf16x8 __attribute__((ext_vector_type(8)));
typedef __bf16 bf16x2 __attribute__((ext_vector_type(2)));
typedef short s16x4 __attribute__((ext_vector_type(4)));
typedef short s16x8 __attribute__((ext_vector_type(8)));
typedef float f32x2 __attribute__((ext_vector_type(2)));
typedef float f32x16 __attribute__((ext_vector_type(16)));
typedef u32 u32x4 __attribute__((ext_vector_type(4)));
#define LAS __attribute__((address_space(3)))
#define GAS __attribute__((address_space(1)))
#define DI __device__ __forceinline__
typedef __amdgpu_buffer_rsrc_t rsrc_t;
typedef GAS u64 gu64;

constexpr int NB = 8, SQ = 128, DM = 640, NH = 32, SK = 512, DH = 192, HD = NH * DH;
constexpr u32 SLOT = 24576;
constexpr u32 WQS = 12288;
constexpr u32 XOFF = 49152;
constexpr u32 WOS = 20480;
constexpr u32 LDS_BYTES = 81920;
constexpr size_t WS_E = 0;
constexpr size_t WS_ENTER = 4096;
constexpr size_t WS_FO = 32768;
constexpr size_t WS_FP = 65536;
#define WS_BASE_MB 208
constexpr size_t WS_O = 131072 + (size_t)WS_BASE_MB * 1048576;
constexpr size_t WS_O_BYTES = (size_t)2 * NB * NH * 4 * 12288;
constexpr size_t WS_P = WS_O + WS_O_BYTES;
constexpr size_t WS_P_BYTES = (size_t)2 * NB * 4 * 4 * 10 * 4 * 1024;

struct Params {
  const void* x[2]; const void* kc[2]; const void* vc[2]; const void* wq[2]; const void* wo[2];
  void* out; void* ws;
};

DI u32 rfl(u32 v) { return __builtin_amdgcn_readfirstlane(v); }
DI rsrc_t mk_rsrc(const void* p, u32 bytes) { return __builtin_amdgcn_make_buffer_rsrc((void*)p, (short)0, (int)bytes, 0x00020000); }
DI u32 cvtpk(float lo, float hi) { f32x2 v = {lo, hi}; bf16x2 b = __builtin_convertvector(v, bf16x2); return __builtin_bit_cast(u32, b); }
DI bf16x8 pack8(const f32x16& a, int base, float sc) {
  u32x4 w = {cvtpk(a[base + 0] * sc, a[base + 1] * sc), cvtpk(a[base + 2] * sc, a[base + 3] * sc), cvtpk(a[base + 4] * sc, a[base + 5] * sc), cvtpk(a[base + 6] * sc, a[base + 7] * sc)};
  return __builtin_bit_cast(bf16x8, w);
}
DI bf16x8 pack8n(const f32x16& a, int base) {
  u32x4 w = {cvtpk(a[base + 0], a[base + 1]), cvtpk(a[base + 2], a[base + 3]), cvtpk(a[base + 4], a[base + 5]), cvtpk(a[base + 6], a[base + 7])};
  return __builtin_bit_cast(bf16x8, w);
}
#define MFMA(a, b, c) __builtin_amdgcn_mfma_f32_32x32x16_bf16((a), (b), (c), 0, 0, 0)
#define PIN() __builtin_amdgcn_sched_barrier(0)
#define MF(acc, a, b) do { acc = MFMA((a), (b), acc); asm volatile("" : "+v"(acc)); } while (0)

template <bool NT> DI void dma16(u32 lds, u32 voff, rsrc_t srd, u32 soff) {
  if constexpr (NT) asm volatile("s_mov_b32 m0, %0\n\ts_nop 0\n\tbuffer_load_dwordx4 %1, %2, %3 offen nt lds" :: "s"(lds), "v"(voff), "s"(srd), "s"(soff));
  else              asm volatile("s_mov_b32 m0, %0\n\ts_nop 0\n\tbuffer_load_dwordx4 %1, %2, %3 offen lds" :: "s"(lds), "v"(voff), "s"(srd), "s"(soff));
}
template <bool NT> DI void pf4(u32 lds, u32 voff, rsrc_t srd) {
  if constexpr (NT) asm volatile("s_mov_b32 m0, %0\n\ts_nop 0\n\tbuffer_load_dword %1, %2, 0 offen nt lds" :: "s"(lds), "v"(voff), "s"(srd));
  else              asm volatile("s_mov_b32 m0, %0\n\ts_nop 0\n\tbuffer_load_dword %1, %2, 0 offen lds" :: "s"(lds), "v"(voff), "s"(srd));
}
DI void set_m0(u32 base, int imm) { asm volatile("s_add_u32 m0, %0, %1" :: "s"(base), "i"(imm) : "scc"); }
template <bool NT> DI void dma_go(u32 voff, rsrc_t srd, u32 soff) {
  if constexpr (NT) asm volatile("buffer_load_dwordx4 %0, %1, %2 offen nt lds" :: "v"(voff), "s"(srd), "s"(soff));
  else              asm volatile("buffer_load_dwordx4 %0, %1, %2 offen lds" :: "v"(voff), "s"(srd), "s"(soff));
}
template <int N> DI void wait_lgkm() { __builtin_amdgcn_s_waitcnt(0xC07F | (N << 8)); }
DI float max3(float a, float b, float c) { return __builtin_elementwise_maximum(__builtin_elementwise_maximum(a, b), c); }
template <int IMM> DI void ld16_sc1(u32x4& dst, u32 voff, rsrc_t srd, u32 soff) {
  asm volatile("buffer_load_dwordx4 %0, %1, %2, %3 offen offset:%4 sc1" : "=v"(dst) : "v"(voff), "s"(srd), "s"(soff), "i"(IMM));
}
template <int IMM> DI void st16_sc1(const u32x4& src, u32 voff, rsrc_t srd, u32 soff) {
  asm volatile("buffer_store_dwordx4 %0, %1, %2, %3 offen offset:%4 sc1\n\ts_nop 1" :: "v"(src), "v"(voff), "s"(srd), "s"(soff), "i"(IMM) : "memory");
}
template <int IMM> DI void st16_o(const u32x4& src, u32 voff, rsrc_t srd, u32 soff) {
  st16_sc1<IMM>(src, voff, srd, soff);
}
DI void keep(u32x4& a) { asm volatile("" : "+v"(a)); }
template <int N> DI void wait_vm(u32x4& a) { asm volatile("s_waitcnt vmcnt(%1)" : "+v"(a) : "i"(N)); }
template <int N> DI void wait_vm(u64& a) { asm volatile("s_waitcnt vmcnt(%1)" : "+v"(a) : "i"(N)); }
DI void ld8_sc1(u64& dst, u32 voff, gu64* base) { asm volatile("global_load_dwordx2 %0, %1, %2 sc1" : "=v"(dst) : "v"(voff), "s"(base)); }
template <int K> DI void flags_again(u64& fl, u32 voff, gu64* base, u64 tag) {
  u32 cnt;
  asm volatile("s_waitcnt vmcnt(%5)\n\tv_cmp_ne_u64_e32 vcc, %4, %0\n\ts_cbranch_vccz 2f\n\ts_mov_b32 %1, 0x40000\n"
               "1:\n\tglobal_load_dwordx2 %0, %2, %3 sc1\n\ts_waitcnt vmcnt(0)\n\tv_cmp_ne_u64_e32 vcc, %4, %0\n\ts_cbranch_vccz 2f\n\t"
               "s_sleep 2\n\ts_sub_u32 %1, %1, 1\n\ts_cmp_lg_u32 %1, 0\n\ts_cbranch_scc1 1b\n2:"
               : "+v"(fl), "=&s"(cnt) : "v"(voff), "s"(base), "s"(tag), "i"(K) : "vcc", "scc", "memory");
}
DI void head_again(u32x4* f, u32 early, u32 fvoff, gu64* base, u64 tag, u32 voff, rsrc_t srd, u32 s0, u32 s1, u32 s2) {
  u32 cnt; u64 fl;
  asm volatile("s_cmp_lg_u32 %[early], 0\n\ts_cbranch_scc1 3f\n\ts_mov_b32 %[cnt], 0x40000\n"
               "1:\n\tglobal_load_dwordx2 %[fl], %[fvoff], %[base] sc1\n\ts_waitcnt vmcnt(0)\n\tv_cmp_ne_u64_e32 vcc, %[tag], %[fl]\n\ts_cbranch_vccz 2f\n\t"
               "s_sleep 2\n\ts_sub_u32 %[cnt], %[cnt], 1\n\ts_cmp_lg_u32 %[cnt], 0\n\ts_cbranch_scc1 1b\n"
               "2:\n\t"
               "buffer_load_dwordx4 %0, %[voff], %[srd], %[s0] offen sc1\n\tbuffer_load_dwordx4 %1, %[voff], %[srd], %[s0] offen offset:1024 sc1\n\t"
               "buffer_load_dwordx4 %2, %[voff], %[srd], %[s0] offen offset:2048 sc1\n\tbuffer_load_dwordx4 %3, %[voff], %[srd], %[s0] offen offset:3072 sc1\n\t"
               "buffer_load_dwordx4 %4, %[voff], %[srd], %[s1] offen sc1\n\tbuffer_load_dwordx4 %5, %[voff], %[srd], %[s1] offen offset:1024 sc1\n\t"
               "buffer_load_dwordx4 %6, %[voff], %[srd], %[s1] offen offset:2048 sc1\n\tbuffer_load_dwordx4 %7, %[voff], %[srd], %[s1] offen offset:3072 sc1\n\t"
               "buffer_load_dwordx4 %8, %[voff], %[srd], %[s2] offen sc1\n\tbuffer_load_dwordx4 %9, %[voff], %[srd], %[s2] offen offset:1024 sc1\n\t"
               "buffer_load_dwordx4 %10, %[voff], %[srd], %[s2] offen offset:2048 sc1\n\tbuffer_load_dwordx4 %11, %[voff], %[srd], %[s2] offen offset:3072 sc1\n\t"
               "s_waitcnt vmcnt(0)\n3:"
               : "+v"(f[0]), "+v"(f[1]), "+v"(f[2]), "+v"(f[3]), "+v"(f[4]), "+v"(f[5]), "+v"(f[6]), "+v"(f[7]), "+v"(f[8]), "+v"(f[9]), "+v"(f[10]), "+v"(f[11]),
                 [cnt] "=&s"(cnt), [fl] "=&v"(fl)
               : [early] "s"(early), [fvoff] "v"(fvoff), [base] "s"(base), [tag] "s"(tag), [voff] "v"(voff), [srd] "s"(srd), [s0] "s"(s0), [s1] "s"(s1), [s2] "s"(s2)
               : "vcc", "scc", "memory");
}
DI void st_out(char* ptr, const u32x4& v) { *(GAS u32x4*)ptr = v; }
template <int N> DI void wait_bar() { asm volatile("s_waitcnt vmcnt(%0)\n\ts_barrier" :: "i"(N) : "memory"); }
template <int N> DI void wait_bar_lgkm() {
  asm volatile("" ::: "memory");
  __builtin_amdgcn_s_waitcnt(0x0070 | (N & 15) | ((N >> 4) << 14));
  asm volatile("s_barrier" ::: "memory");
}
DI bf16x8 lds_b128(u32 addr) { return *(const LAS bf16x8*)(size_t)addr; }
DI s16x4 lds_tr(u32 a) { return __builtin_amdgcn_ds_read_tr16_b64_v4i16((LAS s16x4*)(size_t)a); }
DI bf16x8 comb(s16x4 lo, s16x4 hi) { s16x8 v = __builtin_shufflevector(lo, hi, 0, 1, 2, 3, 4, 5, 6, 7); return __builtin_bit_cast(bf16x8, v); }
DI void put_tag(gu64* word, u64 tag) { __hip_atomic_store(word, tag, __ATOMIC_RELAXED, __HIP_MEMORY_SCOPE_AGENT); }
template <int SLEEP = 2> DI void poll_tags(gu64* words, u32 nmask, u32 lane, u64 tag) {
  u32 spins = 0;
  gu64* wp = words + (lane & nmask);
  while (true) {
    const u64 v = __hip_atomic_load(wp, __ATOMIC_RELAXED, __HIP_MEMORY_SCOPE_AGENT);
    if (__builtin_amdgcn_ballot_w64(v != tag) == 0ull) break;
    __builtin_amdgcn_s_sleep(SLEEP);
    if (++spins > (1u << 18)) break;
  }
}

#define KV_NT 1
#define W_NT 0
#define WO_NT 0
#define X_NT 0
#define USE_PRIO 1
#define PRIO_QA 2
#define PRIO_QB 3
#define PRIO_AA 1
#define PRIO_AB 2
#define PRIO_OA 3
#define PRIO_OB 0
#define OP_BAR 15
#define OP_L1 3
#define OP_L2 4
#define OP_FLT 9

struct OpPlan {
  int kb[24];
  int ko[24][4];
  int ld[24][2];
  int kf, first_s;
  int put;
};
constexpr int op_pieces(int f) { return f < OP_BAR ? 0 : (5 / (20 - OP_BAR)) + ((f - OP_BAR) < 5 % (20 - OP_BAR) ? 1 : 0); }
constexpr int op_ldpos(int nld, int f) {
  return nld == 1 ? ((f & 3) == 1 && f < 16 ? f >> 2 : -1) : nld == 2 ? ((f & 1) == 1 && f < 16 ? f >> 1 : -1) : -1; }
constexpr int own_wo3(int n, int f) { return n == 0 && (f & 1) && f < 10 ? f >> 1 : -1; }
constexpr int own_ld(int n, int f) { return n == 1 || n == 2 ? ((f & 3) == 1 && f < 16 ? f >> 2 : -1) : -1; }
constexpr int own_st(int n, int f) { return n == 0 ? (f == 4 ? 0 : f == 8 ? 1 : f == 12 ? 2 : f == 14 ? 3 : -1) : n < 3 ? (f == 3 ? 0 : f == 7 ? 1 : f == 11 ? 2 : f == 14 ? 3 : -1) : -1; }
constexpr OpPlan make_plan() {
  OpPlan P{};
  int c = 0, posW[28] = {}, posO[24][4] = {}, posF = 0, posS = 0;
  posW[1] = 4; c = 5;
  c += 5; posW[2] = c - 1;
  for (int q = 0; q < 4; ++q) posO[3][q] = c++;
  c += 12;
  P.first_s = 15 - OP_L2; P.put = -1;
  for (int n = 0; n < 24; ++n) {
    int k = 0; P.ld[n][0] = P.ld[n][1] = -1;
    for (int t = 6; t < 24; ++t) if (t - (t < 15 ? OP_L1 : OP_L2) == n) P.ld[n][k++] = t;
    if (n == P.first_s) P.kf = c - 1 - posF;
    for (int f = 0; f < 20; ++f) {
      if (f % 5 == 0) { const int y = n >= 3 ? c - 1 - posO[n][f / 5] : 63; P.ko[n][f / 5] = y > 63 ? 63 : y; }
      if (f == OP_BAR && n < 23) {
        const int y = c - 1 - posW[n + 1]; P.kb[n] = y > 63 ? 63 : y;
        if (P.put < 0 && n >= 2 && posW[n + 1] > posS) P.put = n;
        if (n == OP_FLT) posF = c++;
      }
      const int lp = op_ldpos(k, f);
      if (lp >= 0) posO[P.ld[n][lp >> 2]][lp & 3] = c++;
      if (own_wo3(n, f) >= 0) posW[3] = c++;
      if (own_ld(n, f) >= 0) posO[3 + n][own_ld(n, f)] = c++;
      if (own_st(n, f) >= 0) posS = c++;
      if (n + 4 < 24) { c += op_pieces(f); if (op_pieces(f)) posW[n + 4] = c - 1; }
    }
  }
  return P;
}
constexpr OpPlan PLAN = make_plan();
static_assert(PLAN.put == 5 && PLAN.put < OP_FLT, "");

__global__ __launch_bounds__(256, 2) void fused_kernel(Params p) {
  __shared__ __attribute__((aligned(1024))) char smem[LDS_BYTES];
  const u32 ldsb = (u32)(size_t)(LAS char*)smem;
  const u32 tid = threadIdx.x, lane = tid & 63u, w = rfl(tid >> 6);
  const u32 r = lane & 31u, hh = lane >> 5;
  const u32 blk = blockIdx.x, role = blk >> 8, idx = blk & 255u, sl = idx >> 3;
  const u32 br = (idx >> 2) & 1u, j8 = idx & 3u, b = sl >> 2, i = sl & 3u, j = 2u * j8 + role, h = 4u * j + i;
  char* const wsb = (char*)p.ws;
  if (USE_PRIO) { if (role == 0) __builtin_amdgcn_s_setprio(PRIO_QA); else __builtin_amdgcn_s_setprio(PRIO_QB); }
  u64 ev;
  { const u32 vz = 0; asm volatile("global_load_dwordx2 %0, %1, %2 sc1" : "=v"(ev) : "v"(vz), "s"(wsb)); }

  const rsrc_t srdW = mk_rsrc((const char*)p.wq[br] + h * (DH * 2), (u32)(DM * HD * 2) - h * (DH * 2));
  const rsrc_t srdX = mk_rsrc((const char*)p.x[br] + (size_t)b * SQ * DM * 2, SQ * DM * 2);
  const rsrc_t srdK = mk_rsrc((const char*)p.kc[br] + (size_t)(b * NH + h) * SK * DH * 2, SK * DH * 2);
  const rsrc_t srdV = mk_rsrc((const char*)p.vc[br] + (size_t)(b * NH + h) * SK * DH * 2, SK * DH * 2);

  u32 voffW[3], voffK[3], voffV[3], voffX[2];
#pragma unroll
  for (int v = 0; v < 3; ++v) {
    const u32 z = 64u * v + lane, rr = z / 24u, ph = z % 24u;
    const u32 cV = ph ^ (((rr >> 1) & 1u) << 2);
    const u32 cK = (ph & ~7u) | ((ph & 7u) ^ (((w & 1u) << 2) | (rr >> 1)));
    voffW[v] = rr * (HD * 2) + cV * 16u;
    voffV[v] = rr * 384u + cV * 16u;
    voffK[v] = rr * 384u + cK * 16u;
  }
#pragma unroll
  for (int v = 0; v < 2; ++v) voffX[v] = (lane >> 3) * (DM * 2) + ((lane & 7u) ^ ((u32)(v << 2) | (lane >> 4))) * 16u;
  const u32 blkb = (lane >> 4) & 1u, q4 = (lane & 15u) >> 2, pp = lane & 3u, e = (q4 >> 1) & 1u;
  const u32 bW0 = ldsb + (8u * hh + q4) * 384u + (4u * e + 2u * blkb + (pp & 1u)) * 16u + 8u * (pp >> 1), bW1 = bW0 ^ 64u;
  const u32 baseV0 = ldsb + 12288u + (4u * hh + q4) * 384u + (4u * e + 2u * blkb + (pp >> 1)) * 16u + 8u * (pp & 1u);
  const u32 baseK0 = ldsb + r * 384u + ((hh ^ ((r >> 1) & 7u)) << 4);
  const u32 baseX0 = ldsb + XOFF + w * 4096u + r * 128u + ((hh ^ ((r >> 1) & 7u)) << 4);

  const u32 c0 = (b + (b >> 2) + 2u * i + (i >> 1)) % 10u;
  auto chunk_of = [&](u32 c) { u32 t = c + c0; return t >= 10u ? t - 10u : t; };
  auto wq_piece = [&](u32 slot, u32 so, int v) { dma16<W_NT>(ldsb + slot * WQS + w * 3072u + v * 1024u, voffW[v], srdW, so); };
  auto wq_so = [&](u32 t) { return (chunk_of(t >> 1) * 64u + (t & 1u) * 32u + 8u * w) * (HD * 2); };
  auto x_piece = [&](u32 par, u32 so, int v) { dma16<X_NT>(ldsb + XOFF + par * 16384u + w * 4096u + v * 1024u, voffX[v & 1], srdX, so + v * 8u * (DM * 2)); };
  auto x_so = [&](u32 c) { return 32u * w * (DM * 2) + chunk_of(c) * 128u; };
  auto k_piece = [&](u32 t, u32 slot_off, int v) { dma16<KV_NT>(ldsb + slot_off + w * 3072u + v * 1024u, voffK[v], srdK, t * 12288u + w * 3072u); };
  auto v_piece = [&](u32 t, u32 slot_off, int v) { dma16<KV_NT>(ldsb + slot_off + 12288u + w * 3072u + v * 1024u, voffV[v], srdV, t * 12288u + w * 3072u); };

  f32x16 acc[6];
#pragma unroll
  for (int t = 0; t < 6; ++t) acc[t] = f32x16{};
  if (role == 1) asm volatile("s_waitcnt vmcnt(0)" ::: "memory");
  {
    const u32 s0 = wq_so(0), s1 = wq_so(1), s2 = wq_so(2), s3 = wq_so(3), x0 = x_so(0), x1 = x_so(1);
#pragma unroll
    for (int v = 0; v < 3; ++v) wq_piece(0, s0, v);
#pragma unroll
    for (int v = 0; v < 4; ++v) x_piece(0, x0, v);
#pragma unroll
    for (int v = 0; v < 3; ++v) wq_piece(1, s1, v);
#pragma unroll
    for (int v = 0; v < 3; ++v) wq_piece(2, s2, v);
#pragma unroll
    for (int v = 0; v < 4; ++v) x_piece(1, x1, v);
#pragma unroll
    for (int v = 0; v < 3; ++v) wq_piece(3, s3, v);
  }
  asm volatile("s_waitcnt vmcnt(%1)" : "+v"(ev) : "i"(20) : "memory");
  const u64 tag = (((u64)rfl((u32)(ev >> 32)) << 32) | rfl((u32)ev)) + 1ull;
  asm volatile("s_waitcnt vmcnt(%0)\n\ts_barrier" :: "i"(13) : "memory");
  if (tid == 0) put_tag((gu64*)(wsb + WS_ENTER) + blk, tag);
  auto rdA = [&](int slot, int ks, int m, int hi) {
    return lds_tr(((m & 1) ? bW1 : bW0) + slot * WQS + ks * (16 * 384) + (m >> 1) * 128 + hi * (4 * 384)); };
  auto rdX = [&](int par, int xks) { return lds_b128((baseX0 ^ (u32)(xks << 5)) + par * 16384); };
  s16x4 nlo[6], nhi[6];
  bf16x8 Xn;
  auto qp_ks0 = [&](auto Sc, const s16x4 (&lo)[6], const s16x4 (&hi)[6], const bf16x8& X) {
    constexpr int S = decltype(Sc)::value, PAR = S >> 1, XK = (S & 1) * 2;
    MF(acc[0], comb(lo[0], hi[0]), X); nlo[0] = rdA(S, 1, 0, 0); nhi[0] = rdA(S, 1, 0, 1); nlo[1] = rdA(S, 1, 1, 0); PIN();
    MF(acc[1], comb(lo[1], hi[1]), X); nhi[1] = rdA(S, 1, 1, 1); nlo[2] = rdA(S, 1, 2, 0); nhi[2] = rdA(S, 1, 2, 1); PIN();
    MF(acc[2], comb(lo[2], hi[2]), X); nlo[3] = rdA(S, 1, 3, 0); nhi[3] = rdA(S, 1, 3, 1); nlo[4] = rdA(S, 1, 4, 0); PIN();
    MF(acc[3], comb(lo[3], hi[3]), X); nhi[4] = rdA(S, 1, 4, 1); nlo[5] = rdA(S, 1, 5, 0); nhi[5] = rdA(S, 1, 5, 1); PIN();
    MF(acc[4], comb(lo[4], hi[4]), X); Xn = rdX(PAR, XK + 1); PIN();
    MF(acc[5], comb(lo[5], hi[5]), X); PIN();
  };
  {
    s16x4 Alo[6], Ahi[6];
#pragma unroll
    for (int m = 0; m < 6; ++m) { Alo[m] = rdA(0, 0, m, 0); Ahi[m] = rdA(0, 0, m, 1); }
    const bf16x8 X = rdX(0, 0);
    PIN();
    qp_ks0(std::integral_constant<int, 0>{}, Alo, Ahi, X);
  }
  auto qp_tile = [&](auto TTc, auto LASTc, u32 per) {
    constexpr int TT = decltype(TTc)::value; constexpr bool LAST = decltype(LASTc)::value;
    constexpr int PAR = TT >> 1;
    if constexpr (!LAST) { if constexpr (TT & 1) wait_bar_lgkm<6>(); else wait_bar_lgkm<10>(); }
    else { if constexpr (TT == 0) wait_bar_lgkm<10>(); else if constexpr (TT < 3) wait_bar_lgkm<6>(); else wait_bar_lgkm<63>(); }
    PIN();
    const u32 tn = 4u * per + TT + 4u;
    u32 sw = 0, sx = 0;
    if constexpr (!LAST) { sw = wq_so(tn); if constexpr (TT & 1) sx = x_so(tn >> 1); }
    constexpr int NS = (TT + 1) & 3, NPAR = NS >> 1, NXK = (NS & 1) * 2;
    constexpr bool MORE = !(LAST && TT == 3);
    s16x4 Alo[6], Ahi[6]; bf16x8 X;
#pragma unroll
    for (int m = 0; m < 6; ++m) {
      MF(acc[m], comb(nlo[m], nhi[m]), Xn);
      if constexpr (MORE) { Alo[m] = rdA(NS, 0, m, 0); Ahi[m] = rdA(NS, 0, m, 1); if (m == 0) X = rdX(NPAR, NXK); }
      if constexpr (!LAST) {
        if constexpr (TT & 1) {
          if (m == 0) { x_piece(PAR, sx, 0); } if (m == 1) { x_piece(PAR, sx, 1); x_piece(PAR, sx, 2); } if (m == 2) x_piece(PAR, sx, 3);
          if (m >= 3) wq_piece(TT, sw, m - 3);
        } else { if (m & 1) wq_piece(TT, sw, m >> 1); }
      } else {
        if constexpr (TT == 0) { if (m & 1) k_piece(0, 0, m >> 1); }
        if constexpr (TT == 1) { if (m & 1) v_piece(0, 0, m >> 1); }
        if constexpr (TT == 2) { if (m < 3) k_piece(1, SLOT, m); else k_piece(2, 2 * SLOT, m - 3); }
        if constexpr (TT == 3) { if (m & 1) v_piece(1, SLOT, m >> 1); }
      }
      PIN();
    }
    if constexpr (MORE) qp_ks0(std::integral_constant<int, NS>{}, Alo, Ahi, X);
  };
  using std::integral_constant;
#define IC(n) integral_constant<int, n>{}
#define BC(n) integral_constant<bool, n>{}
  for (u32 per = 0; per < 4; ++per) { qp_tile(IC(0), BC(false), per); qp_tile(IC(1), BC(false), per); qp_tile(IC(2), BC(false), per); qp_tile(IC(3), BC(false), per); }
  qp_tile(IC(0), BC(true), 4); qp_tile(IC(1), BC(true), 4); qp_tile(IC(2), BC(true), 4); qp_tile(IC(3), BC(true), 4);
  constexpr float CSC = 0.07216878364870322f * 1.4426950408889634f;
  bf16x8 qf[12];
#pragma unroll
  for (int t = 0; t < 6; ++t) { qf[2 * t] = pack8(acc[t], 0, CSC); qf[2 * t + 1] = pack8(acc[t], 8, CSC); }
  if (USE_PRIO) { if (role == 0) __builtin_amdgcn_s_setprio(PRIO_AA); else __builtin_amdgcn_s_setprio(PRIO_AB); }

  const rsrc_t srdO = mk_rsrc((const char*)p.wo[br] + ((size_t)(1536u * j8) * DM + 160u * i) * 2, role == 0 ? (u32)(HD * DM * 2) - (1536u * j8 * DM + 160u * i) * 2u : 0u);
  u32 voffO[5];
#pragma unroll
  for (int v = 0; v < 5; ++v) { const u32 z = 64u * v + lane; voffO[v] = (z / 20u) * (DM * 2) + (z % 20u) * 16u; }
  auto wo_so = [&](u32 half, u32 n) { u32 t = n + 3u * i; t = t >= 12u ? t - 12u : t; return ((half * 12u + t) * 64u + 16u * w) * (DM * 2); };
  auto wo_piece = [&](int n, u32 so, int v) { dma16<WO_NT>(ldsb + ((n + 2) & 3) * WOS + w * 5120u + v * 1024u, voffO[v], srdO, so); };

  constexpr float THR = 8.0f;
  f32x16 o[6];
#pragma unroll
  for (int t = 0; t < 6; ++t) o[t] = f32x16{};
  wait_bar<12>();
  u32 kb[4], vb[2];
#pragma unroll
  for (int q = 0; q < 4; ++q) kb[q] = baseK0 ^ (u32)(q << 5);
  vb[0] = baseV0; vb[1] = baseV0 ^ 64u;
  auto rdK = [&](int slot, int st) { return lds_b128(kb[st & 3] + slot * SLOT + (st >> 2) * 128); };
  struct VF { s16x4 lo, hi; };
  auto rdV = [&](int slot, int n) { const int t = n % 6, hf = n / 6; const u32 a = vb[t & 1] + slot * SLOT + (t >> 1) * 128 + hf * (16 * 384); VF f; f.lo = lds_tr(a); f.hi = lds_tr(a + 8 * 384); return f; };
  const u32 dmaB = ldsb + w * 3072u;
  const rsrc_t srdV2 = mk_rsrc((const char*)p.vc[br] + (size_t)(b * NH + h) * SK * DH * 2 - 2 * 12288, SK * DH * 2 + 2 * 12288);
  u32 soK = 3 * 12288u + w * 3072u;
  f32x16 sa, sb, negm;
  float l = 0.f;
  bf16x8 kw[4];
  {
    bf16x8 kf[12];
#pragma unroll
    for (int n = 0; n < 6; ++n) kf[n] = rdK(0, n);
    PIN();
    sa = f32x16{};
#pragma unroll
    for (int n = 0; n < 12; ++n) { MF(sa, kf[n], qf[n]); if (n < 6) kf[n + 6] = rdK(0, n + 6); PIN(); }
    wait_bar_lgkm<6>();
#pragma unroll
    for (int v = 0; v < 3; ++v) dma16<KV_NT>(dmaB + v * 1024u, voffK[v], srdK, soK);
    soK += 12288u;
#pragma unroll
    for (int n = 0; n < 4; ++n) kw[n] = rdK(1, n);
    float pm = max3(sa[0], sa[1], sa[2]);
#pragma unroll
    for (int k = 3; k < 15; k += 2) pm = max3(pm, sa[k], sa[k + 1]);
    pm = __builtin_elementwise_maximum(pm, sa[15]);
    { auto rr = __builtin_amdgcn_permlane32_swap(__float_as_uint(pm), __float_as_uint(pm), false, false); pm = __builtin_elementwise_maximum(__uint_as_float(rr[0]), __uint_as_float(rr[1])); }
#pragma unroll
    for (int k = 0; k < 16; ++k) { sa[k] -= pm; negm[k] = -pm; }
    asm volatile("" : "+v"(negm));
    wait_lgkm<2>();
    PIN();
  }
  gu64* const FO = (gu64*)(wsb + WS_FO + (((br * NB + b) * 8u + j) * 4u + w) * 64u);
  u64 flg;
  auto att_body = [&](auto MODEc, auto S3c, f32x16& sc, f32x16& sn) {
    constexpr int MODE = decltype(MODEc)::value;
    constexpr int S0 = decltype(S3c)::value, S1 = (S0 + 1) % 3, S2 = (S0 + 2) % 3;
    constexpr bool QK = MODE < 15, KNEXT = MODE < 14, DV = MODE < 14, DK = MODE == 0, WO = MODE >= 14;
    u32 pw[8];
    auto E = [&](int k) { sc[k] = __builtin_amdgcn_exp2f(sc[k]); };
    auto C = [&](int q) { pw[q] = cvtpk(sc[2 * q], sc[2 * q + 1]); };
    auto A = [&](int k) { l += sc[k]; };
    bf16x8 kf[12];
    VF vf[12];
    u32 sow = 0;
    if constexpr (WO) sow = wo_so(role, MODE - 14);
    auto D0 = [&](int q) {
      if constexpr (WO) { if (q < 5) set_m0(ldsb + w * 5120u, ((MODE - 14 + 2) & 3) * WOS + q * 1024); }
      else { if (q < 3) { if (DV) set_m0(dmaB, S2 * SLOT + 12288 + q * 1024); } else { if (DK) set_m0(dmaB, S1 * SLOT + (q - 3) * 1024); } }
    };
    auto D1 = [&](int q) {
      if constexpr (WO) { if (q < 5) dma_go<WO_NT>(voffO[q], srdO, sow); }
      else { if (q < 3) { if (DV) dma_go<KV_NT>(voffV[q], srdV2, soK); } else { if (DK) dma_go<KV_NT>(voffK[q - 3], srdK, soK); } }
    };
    float pm = 0.f;
    auto M = [&](int q) { if constexpr (QK) { if (q == 0) pm = max3(sn[0], sn[1], sn[2]); else if (q < 7) pm = max3(pm, sn[2 * q + 1], sn[2 * q + 2]); else pm = __builtin_elementwise_maximum(pm, sn[15]); } };
#pragma unroll
    for (int n = 0; n < 4; ++n) kf[n] = kw[n];
#pragma unroll
    for (int n = 0; n < 12; ++n) {
      if constexpr (QK) {
        if (n == 0) sn = MFMA(kf[0], qf[0], negm); else sn = MFMA(kf[n], qf[n], sn);
        asm volatile("" : "+v"(sn));
        if (n < 8) kf[n + 4] = rdK(S1, n + 4);
      }
      if (n < 8) {
        if ((n & 1) == 0) { E(n); E(n + 1); }
        else { C(n >> 1); A(n - 1); A(n); if constexpr (QK) wait_lgkm<2>(); }
      }
      if (n == 8) { E(8); E(9); }
      if (n == 9) {
        if constexpr (MODE <= 12) wait_bar_lgkm<6>(); else if constexpr (MODE <= 14) wait_bar_lgkm<3>(); else wait_bar_lgkm<5>();
        if constexpr (MODE == 15) ld8_sc1(flg, (lane & 3u) * 8u, FO + 32);
        if constexpr (MODE == 15)
          pf4<false>(ldsb + SLOT + w * 256u, (240u * sl + (tid < 240u ? tid : 0u)) * 128u, mk_rsrc((const char*)p.wo[br] + (size_t)(1536u * j8 + 768u * (role ^ 1u)) * DM * 2, 768 * DM * 2));
        vf[0] = rdV(S0, 0); vf[1] = rdV(S0, 1);
      }
      if (n == 10) { vf[2] = rdV(S0, 2); D0(0); C(4); E(10); }
      if (n == 11) { vf[3] = rdV(S0, 3); D1(0); E(11); wait_lgkm<4>(); }
      PIN();
    }
    const bf16x8 pf0 = __builtin_bit_cast(bf16x8, u32x4{pw[0], pw[1], pw[2], pw[3]});
    bool resc = false;
#pragma unroll
    for (int n = 0; n < 12; ++n) {
      if (n < 6) { MF(o[n], comb(vf[n].lo, vf[n].hi), pf0); }
      else { const bf16x8 pf1 = __builtin_bit_cast(bf16x8, u32x4{pw[4], pw[5], pw[6], pw[7]}); MF(o[n - 6], comb(vf[n].lo, vf[n].hi), pf1); }
      if (n < 8) vf[n + 4] = rdV(S0, n + 4);
      else if constexpr (KNEXT) kw[n - 8] = rdK(S2, n - 8);
      if (n == 0) { D0(1); C(5); E(12); }
      if (n == 1) { D1(1); E(13); wait_lgkm<4>(); }
      if (n == 2) { D0(2); C(6); E(14); }
      if (n == 3) { D1(2); E(15); wait_lgkm<4>(); }
      if (n == 4) { D0(3); C(7); M(0); }
      if (n == 5) { D1(3); M(1); wait_lgkm<4>(); }
      if (n == 6) { D0(4); M(2); M(3); }
      if (n == 7) { D1(4); M(4); A(8); wait_lgkm<4>(); }
      if (n == 8) { D0(5); M(5); M(6); A(9); }
      if (n == 9) { D1(5); M(7); A(10); if constexpr (KNEXT) wait_lgkm<2>(); else wait_lgkm<0>(); }
      if (n == 10) { if constexpr (QK) resc = __builtin_amdgcn_ballot_w64(pm > THR) != 0ull; A(11); A(12); A(13); }
      if (n == 11) { A(14); A(15); soK += 12288u; if constexpr (KNEXT) wait_lgkm<2>(); }
      PIN();
    }
    if constexpr (QK) {
      if (__builtin_expect(resc, 0)) {
        auto rr = __builtin_amdgcn_permlane32_swap(__float_as_uint(pm), __float_as_uint(pm), false, false);
        const float dl = __builtin_elementwise_maximum(__builtin_elementwise_maximum(__uint_as_float(rr[0]), __uint_as_float(rr[1])), 0.f);
        const float al = __builtin_amdgcn_exp2f(-dl);
        l *= al;
#pragma unroll
        for (int t = 0; t < 6; ++t)
#pragma unroll
          for (int k = 0; k < 16; ++k) o[t][k] *= al;
#pragma unroll
        for (int k = 0; k < 16; ++k) { sn[k] -= dl; negm[k] -= dl; }
      }
    }
  };
  for (u32 it = 0; it < 12; it += 6) {
    att_body(IC(0), IC(0), sa, sb); att_body(IC(0), IC(1), sb, sa);
    att_body(IC(0), IC(2), sa, sb); att_body(IC(0), IC(0), sb, sa);
    att_body(IC(0), IC(1), sa, sb); att_body(IC(0), IC(2), sb, sa);
  }
  att_body(IC(12), IC(0), sa, sb); att_body(IC(13), IC(1), sb, sa);
  att_body(IC(14), IC(2), sa, sb); att_body(IC(15), IC(0), sb, sa);
  if (USE_PRIO) { if (role == 0) __builtin_amdgcn_s_setprio(PRIO_OA); else __builtin_amdgcn_s_setprio(PRIO_OB); }
  const rsrc_t srdOb = mk_rsrc(wsb + WS_O + (size_t)((br * NB + b) * NH) * 4 * 12288, NH * 4 * 12288);
  const u32 voffL = lane * 16u;
  auto wo_son = [&](int n) { return n < 3 ? wo_so(0u, n) : n < 15 ? wo_so(1u, n - 3) : wo_so(0u, n - 12); };
  u32x4 of[24];
  auto o_so = [&](int n) {
    const u32 head = n < 15 ? 4u * j + 4u + ((i + (u32)((n - 3) / 3)) & 3u) : 4u * j + ((i + 1u + (u32)((n - 15) / 3)) & 3u);
    return (head * 4u + w) * 12288u + (u32)(n % 3) * 4096u; };
  auto ld_frag = [&](auto Nc, auto Qc, rsrc_t srd, u32 so) {
    constexpr int N = decltype(Nc)::value, Q = decltype(Qc)::value;
    ld16_sc1<Q * 1024>(of[4 * ((N - 3) % 6) + Q], voffL, srd, so); };
  auto ld_tile = [&](auto Nc, rsrc_t srd) { const u32 so = o_so(decltype(Nc)::value);
    ld_frag(Nc, IC(0), srd, so); ld_frag(Nc, IC(1), srd, so); ld_frag(Nc, IC(2), srd, so); ld_frag(Nc, IC(3), srd, so); };
  const u32 baseO = ldsb + (4u * hh + q4) * 320u + 32u * blkb + 16u * (pp & 1u) + 8u * (pp >> 1);
  auto rdO = [&](int n, int f, int hi) { return lds_tr(baseO + ((n + 2) & 3) * WOS + (f / 5) * (16 * 320) + (f % 5) * 64 + hi * (8 * 320)); };
  s16x4 wlo[5], whi[5];
  asm volatile("s_waitcnt vmcnt(%1)\n\ts_barrier" : "+v"(flg) : "i"(6) : "memory");
  { auto rr = __builtin_amdgcn_permlane32_swap(__float_as_uint(l), __float_as_uint(l), false, false); l = __uint_as_float(rr[0]) + __uint_as_float(rr[1]); }
  const float inv = 1.0f / l;
  const u32 soOwn = (h * 4u + w) * 12288u;
  u32x4 own[12];
  auto pk_own = [&](int q) { own[q] = __builtin_bit_cast(u32x4, pack8(o[q >> 1], (q & 1) * 8, inv)); };
  auto st_own = [&](auto Qc) { constexpr int Q = decltype(Qc)::value; st16_o<(Q & 3) * 1024>(own[Q], voffL, srdOb, soOwn + (Q >> 2) * 4096u); };
  const u32 early = rfl(role == 0 && __builtin_amdgcn_ballot_w64(flg != tag) == 0ull ? 1u : 0u);
  const rsrc_t srdE = mk_rsrc(wsb + WS_O + (size_t)((br * NB + b) * NH) * 4 * 12288, early ? NH * 4 * 12288 : 0);
  if (role == 0) {
    const u32 so = wo_son(2);
#pragma unroll
    for (int v = 0; v < 5; ++v) wo_piece(2, so, v);
    ld_tile(IC(3), srdE);
  }
  PIN();
  {
    const rsrc_t srdS = mk_rsrc(wsb + WS_O + (size_t)((br * NB + b) * NH) * 4 * 12288, role == 1 ? NH * 4 * 12288 : 0);
    auto ps = [&](auto Qc) { constexpr int Q = decltype(Qc)::value; pk_own(Q); st16_o<(Q & 3) * 1024>(own[Q], voffL, srdS, soOwn + (Q >> 2) * 4096u); };
    ps(IC(0)); ps(IC(1)); ps(IC(2)); ps(IC(3)); ps(IC(4)); ps(IC(5)); ps(IC(6)); ps(IC(7)); ps(IC(8)); ps(IC(9)); ps(IC(10)); ps(IC(11));
  }
  if (role == 1) {
    asm volatile("s_waitcnt vmcnt(0)" ::: "memory");
    if (lane == 0) put_tag(FO + i, tag);
    return;
  }

  f32x16 oa[5];
#pragma unroll
  for (int t = 0; t < 5; ++t) oa[t] = f32x16{};
#pragma unroll
  for (int f = 0; f < 5; ++f) { wlo[f] = rdO(0, f, 0); whi[f] = rdO(0, f, 1); }
  u64 fls;
  PIN();
  auto lane_now = [&]() { u32 t = voffL; asm volatile("" : "+v"(t)); return t >> 4; };
  auto op_tile = [&](auto Nc) {
    constexpr int N = decltype(Nc)::value;
    constexpr int B0 = N < 3 ? 0 : 4 * ((N - 3) % 6);
    constexpr int L0 = PLAN.ld[N][0], L1 = PLAN.ld[N][1], NL = (L0 >= 0) + (L1 >= 0);
    if constexpr (N == 3) {
      head_again(of, early, (lane_now() & 3u) * 8u, FO + 32, tag, voffL, srdOb, o_so(3), o_so(4), o_so(5));
    }
    if constexpr (N == PLAN.first_s) {
      flags_again<PLAN.kf>(fls, (lane_now() & 3u) * 8u, FO, tag);
    }
    u32 so = 0, s0 = 0, s1 = 0;
    if constexpr (L0 >= 0) s0 = o_so(L0);
    if constexpr (L1 >= 0) s1 = o_so(L1);
    auto step = [&](auto Fc) {
      constexpr int f = decltype(Fc)::value;
      if constexpr (N >= 3 && f % 5 == 0 && PLAN.ko[N][f / 5] < 63) wait_vm<PLAN.ko[N][f / 5]>(of[B0 + f / 5]);
      const u32x4 bfr = N < 3 ? own[4 * (N < 3 ? N : 0) + f / 5] : of[B0 + f / 5];
      MF(oa[f % 5], comb(wlo[f % 5], whi[f % 5]), __builtin_bit_cast(bf16x8, bfr));
      if constexpr (f == OP_BAR && N < 23) {
        wait_bar_lgkm<PLAN.kb[N]>();
        if constexpr (N == PLAN.put) { if (voffL == 0) put_tag(FO + i, tag); }
        if constexpr (N == OP_FLT) ld8_sc1(fls, (lane_now() & 3u) * 8u, FO);

        if constexpr (N + 4 < 24) so = wo_son(N + 4);
      }
      if constexpr (f < 15) { wlo[f % 5] = rdO(N, f + 5, 0); whi[f % 5] = rdO(N, f + 5, 1); }
      else if constexpr (N < 23 && f >= OP_BAR) {
#pragma unroll
        for (int q = (f == OP_BAR ? 0 : f - 15); q <= f - 15; ++q) { wlo[q] = rdO(N + 1, q, 0); whi[q] = rdO(N + 1, q, 1); }
      }
      constexpr int lp = op_ldpos(NL, f);
      if constexpr (lp >= 0) {
        if constexpr (lp < 4) ld_frag(IC(L0 < 3 ? 3 : L0), IC(lp & 3), srdOb, s0); else ld_frag(IC(L1 < 3 ? 3 : L1), IC(lp & 3), srdOb, s1); }
      if constexpr (N < 3) {
        if constexpr (own_wo3(N, f) >= 0) wo_piece(3, wo_son(3), own_wo3(N, f));
        if constexpr (own_ld(N, f) >= 0) ld_frag(IC(3 + N), IC(own_ld(N, f) & 3), srdE, o_so(3 + N));
        if constexpr (own_st(N, f) >= 0) st_own(IC(4 * N + (own_st(N, f) & 3)));
      }
      if constexpr (N + 4 < 24 && f >= OP_BAR) {
        constexpr int done = [] { int d = 0; for (int x = 0; x < f; ++x) d += op_pieces(x); return d; }();
#pragma unroll
        for (int v = 0; v < op_pieces(f); ++v) {
          wo_piece(N + 4, so, done + v);
        }
      }
      PIN();
    };
    step(IC(0)); step(IC(1)); step(IC(2)); step(IC(3)); step(IC(4)); step(IC(5)); step(IC(6)); step(IC(7)); step(IC(8)); step(IC(9));
    step(IC(10)); step(IC(11)); step(IC(12)); step(IC(13)); step(IC(14)); step(IC(15)); step(IC(16)); step(IC(17)); step(IC(18)); step(IC(19));
  };
  op_tile(IC(0)); op_tile(IC(1)); op_tile(IC(2)); op_tile(IC(3)); op_tile(IC(4)); op_tile(IC(5)); op_tile(IC(6)); op_tile(IC(7));
  op_tile(IC(8)); op_tile(IC(9)); op_tile(IC(10)); op_tile(IC(11));
  op_tile(IC(12)); op_tile(IC(13)); op_tile(IC(14)); op_tile(IC(15)); op_tile(IC(16)); op_tile(IC(17)); op_tile(IC(18)); op_tile(IC(19));
  op_tile(IC(20)); op_tile(IC(21)); op_tile(IC(22)); op_tile(IC(23));
  const rsrc_t srdP = mk_rsrc(wsb + WS_P + (size_t)((((br * NB + b) * 4u + i) * 4u + w) * 40u) * 1024u, 40 * 1024);
#pragma unroll
  for (int u = 0; u < 10; ++u) st16_sc1<0>(__builtin_bit_cast(u32x4, pack8n(oa[u >> 1], (u & 1) * 8)), voffL, srdP, (u * 4u + j8) * 1024u);
  asm volatile("s_waitcnt vmcnt(0)" ::: "memory");
  gu64* const FP = (gu64*)(wsb + WS_FP + (((br * NB + b) * 4u + i) * 4u + w) * 64u);
  if (voffL == 0) put_tag(FP + j8, tag);
  const u32 ln = lane_now();
  poll_tags(FP, 3u, ln, tag);
  asm volatile("" ::: "memory");
  {
    u32x4 pv[12];
    const u32 so = j8 * 8192u, s2 = (8u + (j8 & 1u)) * 4096u;
    ld16_sc1<0>(pv[0], voffL, srdP, so); ld16_sc1<1024>(pv[1], voffL, srdP, so); ld16_sc1<2048>(pv[2], voffL, srdP, so); ld16_sc1<3072>(pv[3], voffL, srdP, so);
    ld16_sc1<0>(pv[4], voffL, srdP, so + 4096u); ld16_sc1<1024>(pv[5], voffL, srdP, so + 4096u); ld16_sc1<2048>(pv[6], voffL, srdP, so + 4096u); ld16_sc1<3072>(pv[7], voffL, srdP, so + 4096u);
    const u32 vo2 = j8 < 2u ? voffL : 0x7fff0000u;
    ld16_sc1<0>(pv[8], vo2, srdP, s2); ld16_sc1<1024>(pv[9], vo2, srdP, s2); ld16_sc1<2048>(pv[10], vo2, srdP, s2); ld16_sc1<3072>(pv[11], vo2, srdP, s2);
    asm volatile("s_waitcnt vmcnt(0)" : "+v"(pv[0]), "+v"(pv[1]), "+v"(pv[2]), "+v"(pv[3]), "+v"(pv[4]), "+v"(pv[5]), "+v"(pv[6]), "+v"(pv[7]),
                 "+v"(pv[8]), "+v"(pv[9]), "+v"(pv[10]), "+v"(pv[11]));
    auto red = [&](int q) {
      u32x4 ov;
#pragma unroll
      for (int k = 0; k < 4; ++k) {
        float a = __uint_as_float(pv[q][k] << 16), c = __uint_as_float(pv[q][k] & 0xffff0000u);
#pragma unroll
        for (int jj = 1; jj < 4; ++jj) { a += __uint_as_float(pv[q + jj][k] << 16); c += __uint_as_float(pv[q + jj][k] & 0xffff0000u); }
        ov[k] = cvtpk(a, c);
      }
      return ov;
    };
    char* const ob = (char*)p.out + ((size_t)(b * SQ + 32u * w) * (2 * DM) + br * DM + 160u * i) * 2;
    const u32x4 e0 = red(0), e1 = red(4);
    u32x4 f0, f1;
#pragma unroll
    for (int k = 0; k < 4; ++k) { auto rr = __builtin_amdgcn_permlane16_swap(e0[k], e1[k], false, false); f0[k] = rr[0]; f1[k] = rr[1]; }
    char* const orow = ob + (size_t)(ln & 15u) * (4 * DM) + (32u * j8 + 16u * ((ln >> 4) & 1u) + 8u * (ln >> 5)) * 2;
    st_out(orow, f0);
    st_out(orow + 16 * 4 * DM, f1);
    if (j8 < 2u) st_out(ob + (size_t)(ln & 31u) * (4 * DM) + (16u * (8u + j8) + 8u * (ln >> 5)) * 2, red(8));
  }
  if (blk == 0 && w == 0) {
    u32 spins = 0;
    while (spins < (1u << 20)) {
      bool ok = true;
#pragma unroll
      for (int q = 0; q < 8; ++q) ok &= __hip_atomic_load((gu64*)(wsb + WS_ENTER) + q * 64 + ln, __ATOMIC_RELAXED, __HIP_MEMORY_SCOPE_AGENT) == tag;
      if (__builtin_amdgcn_ballot_w64(!ok) == 0ull) break;
      __builtin_amdgcn_s_sleep(8);
      if (++spins > (1u << 16)) break;
    }
    if (ln == 0) put_tag((gu64*)(wsb + WS_E), tag);
  }
}

extern "C" void kernel_launch(void* const* d_in, const int* in_sizes, int n_in, void* d_out, int out_size, void* d_ws, size_t ws_size, hipStream_t stream) {
  Params p;
  for (int br = 0; br < 2; ++br) {
    p.x[br] = d_in[br * 5 + 0]; p.kc[br] = d_in[br * 5 + 1]; p.vc[br] = d_in[br * 5 + 2]; p.wq[br] = d_in[br * 5 + 3]; p.wo[br] = d_in[br * 5 + 4];
  }
  p.out = d_out; p.ws = d_ws;
  hipLaunchKernelGGL(fused_kernel, dim3(512), dim3(256), 0, stream, p);
}
```
